```python
import torch

def _softmax(x, axis=-1):
    return torch.softmax(x, dim=axis)
def _gelu(x):
    return torch.nn.functional.gelu(x, approximate="tanh")
def _silu(x):
    return torch.nn.functional.silu(x)
def _sigmoid(x):
    return torch.sigmoid(x)
def _layernorm(x, axis=-1, eps=1e-5):
    m = x.mean(dim=axis, keepdim=True)
    v = x.var(dim=axis, keepdim=True, unbiased=False)
    return (x - m) / torch.sqrt(v + eps)
def _scatter_add(tgt, idx, src, axis=0):
    return tgt.clone().index_add_(axis, idx, src)
def _segment_sum(src, seg, n):
    out = torch.zeros((n,) + src.shape[1:], dtype=src.dtype)
    return out.index_add_(0, seg, src)
def _pad(x, pw):
    flat = tuple(v for pair in reversed(pw) for v in pair)
    return torch.nn.functional.pad(x, flat)

kernel_name = 'gated_mix__softmax_full_mha__geglu_ffn__prompt'

def setup_inputs():

    _w = (lambda a: (lambda t: t.to(torch.bfloat16) if t.is_floating_point() else t)(torch.as_tensor(a)))
    def _A_setup():
        B, S, D = 1, 2048, 576
        return {
            "x": _w((lambda *s: torch.randn(*s, dtype=torch.bfloat16) * 0.05)(B, S, D)),
            "wqkv": _w((lambda *s: torch.randn(*s, dtype=torch.bfloat16) * 0.8)(D, 3 * D)),
            "wo": _w((lambda *s: torch.randn(*s, dtype=torch.bfloat16) * 0.05)(D, D)),
        }
    _A_in = _A_setup()

    def _B_setup():
        B, S, D, F = 1, 2048, 576, 4608
        return {
            "x": _w((lambda *s: torch.randn(*s, dtype=torch.bfloat16) * 0.05)(B, S, D)),
            "w_gate": _w((lambda *s: torch.randn(*s, dtype=torch.bfloat16) * 0.1)(D, F)),
            "w_up": _w((lambda *s: torch.randn(*s, dtype=torch.bfloat16) * 0.1)(D, F)),
            "w_down": _w((lambda *s: torch.randn(*s, dtype=torch.bfloat16) * 0.1)(F, D)),
        }
    _B_in = _B_setup()

    return {
        "A_x": _A_in["x"],
        "A_wqkv": _A_in["wqkv"],
        "A_wo": _A_in["wo"],
        "B_x": _B_in["x"],
        "B_w_gate": _B_in["w_gate"],
        "B_w_up": _B_in["w_up"],
        "B_w_down": _B_in["w_down"],
        "gate_w": _w((lambda *s: torch.randn(*s, dtype=torch.bfloat16) * 0.05)(576) * 16.0),
    }

def reference(A_x, A_wqkv, A_wo, B_x, B_w_gate, B_w_up, B_w_down, gate_w):
    def _A_ref(x, wqkv, wo):
        B, S, D = x.shape
        H = 32; Dh = D // H
        qkv = (lambda x, *d: x.permute(*d))((x @ wqkv).reshape(B, S, 3, H, Dh), 2, 0, 3, 1, 4)
        q, k, v = qkv[0], qkv[1], qkv[2]
        scale = Dh ** -0.5
        scores = torch.einsum("bhqd,bhkd->bhqk", q, k) * scale
        p = _softmax(scores, axis=-1)
        o = torch.einsum("bhqk,bhkd->bhqd", p, v)
        return (lambda x, *d: x.permute(*d))(o, 0, 2, 1, 3).reshape(B, S, D) @ wo

    def _B_ref(x, w_gate, w_up, w_down):
        return (_gelu(x @ w_gate) * (x @ w_up)) @ w_down

    x = A_x
    g = _sigmoid((x * gate_w).sum(-1, keepdims=True))
    a = _A_ref(x, A_wqkv, A_wo)
    b = _B_ref(x, B_w_gate, B_w_up, B_w_down)
    return g * a + (1 - g) * b
```

```cpp
#include <hip/hip_runtime.h>
#include <hip/hip_bf16.h>
typedef __hip_bfloat16 bf16;
constexpr int S=2048, D=576, H=32, DH=18, F=4608;
__device__ inline float b2f(bf16 v){ return __bfloat162float(v); }
__global__ void gemm_naive(const bf16* A, const bf16* B, float* C, int M,int N,int K){
  __shared__ float As[16][17], Bs[16][17];
  int tx=threadIdx.x, ty=threadIdx.y; int r=blockIdx.y*16+ty, c=blockIdx.x*16+tx; float acc=0;
  for(int k0=0;k0<K;k0+=16){ As[ty][tx]=b2f(A[(long)r*K+k0+tx]); Bs[ty][tx]=b2f(B[(long)(k0+ty)*N+c]); __syncthreads();
    for(int k=0;k<16;k++) acc+=As[ty][k]*Bs[k][tx]; __syncthreads(); }
  C[(long)r*N+c]=acc;
}
__global__ void attn_naive(const float* qkv, bf16* o){
  int h=blockIdx.y; int q=blockIdx.x*64+threadIdx.x;
  float qv[DH]; for(int d=0;d<DH;d++) qv[d]=b2f(__float2bfloat16(qkv[(long)q*3*D+h*DH+d]));
  float m=-1e30f,l=0,acc[DH]; for(int d=0;d<DH;d++) acc[d]=0;
  const float sc=rsqrtf((float)DH);
  for(int k=0;k<S;k++){ float s=0; for(int d=0;d<DH;d++) s+=qv[d]*b2f(__float2bfloat16(qkv[(long)k*3*D+D+h*DH+d])); s*=sc;
    float mn=fmaxf(m,s); float a=__expf(m-mn), p=__expf(s-mn); l=l*a+p; for(int d=0;d<DH;d++) acc[d]=acc[d]*a+p*b2f(__float2bfloat16(qkv[(long)k*3*D+2*D+h*DH+d])); m=mn; }
  for(int d=0;d<DH;d++) o[(long)q*D+h*DH+d]=__float2bfloat16(acc[d]/l);
}
__global__ void geglu(const float* g,const float* u,bf16* h,long n){ long i=blockIdx.x*256L+threadIdx.x; if(i<n){ float x=g[i]; float t=tanhf(0.7978845608f*(x+0.044715f*x*x*x)); h[i]=__float2bfloat16(0.5f*x*(1+t)*u[i]); } }
__global__ void mix(const bf16* x,const bf16* gw,const float* a,const float* b,bf16* out){
  int r=blockIdx.x; __shared__ float red[64]; float s=0; for(int d=threadIdx.x;d<D;d+=64) s+=b2f(x[r*D+d])*b2f(gw[d]);
  red[threadIdx.x]=s; __syncthreads(); float t=0; for(int i=0;i<64;i++) t+=red[i]; float g=1.f/(1.f+__expf(-t));
  for(int d=threadIdx.x;d<D;d+=64) out[r*D+d]=__float2bfloat16(g*a[r*D+d]+(1-g)*b[r*D+d]);
}
extern "C" void kernel_launch(void* const* d_in, const int* in_sizes, int n_in, void* d_out, int out_size, void* d_ws, size_t ws_size, hipStream_t stream){
  const bf16* x=(const bf16*)d_in[0]; const bf16* wqkv=(const bf16*)d_in[1]; const bf16* wo=(const bf16*)d_in[2];
  const bf16* wg=(const bf16*)d_in[4]; const bf16* wu=(const bf16*)d_in[5]; const bf16* wd=(const bf16*)d_in[6]; const bf16* gw=(const bf16*)d_in[7];
  char* ws=(char*)d_ws; float* qkv=(float*)ws; ws+=(size_t)S*3*D*4; float* g=(float*)ws; ws+=(size_t)S*F*4; float* u=(float*)ws; ws+=(size_t)S*F*4;
  bf16* h=(bf16*)ws; ws+=(size_t)S*F*2; bf16* o=(bf16*)ws; ws+=(size_t)S*D*2; float* a=(float*)ws; ws+=(size_t)S*D*4; float* b=(float*)ws;
  dim3 bl(16,16);
  gemm_naive<<<dim3(3*D/16,S/16),bl,0,stream>>>(x,wqkv,qkv,S,3*D,D);
  attn_naive<<<dim3(S/64,H),64,0,stream>>>(qkv,o);
  gemm_naive<<<dim3(D/16,S/16),bl,0,stream>>>(o,wo,a,S,D,D);
  gemm_naive<<<dim3(F/16,S/16),bl,0,stream>>>(x,wg,g,S,F,D);
  gemm_naive<<<dim3(F/16,S/16),bl,0,stream>>>(x,wu,u,S,F,D);
  geglu<<<(S*F+255)/256,256,0,stream>>>(g,u,h,(long)S*F);
  gemm_naive<<<dim3(D/16,S/16),bl,0,stream>>>(h,wd,b,S,D,F);
  mix<<<S,64,0,stream>>>(x,gw,a,b,(bf16*)d_out);
}
```

```cpp
#include <hip/hip_runtime.h>
#include <hip/hip_bf16.h>
#include <stdint.h>

typedef __attribute__((ext_vector_type(8))) short s16x8;
typedef __attribute__((ext_vector_type(4))) short s16x4;
typedef __attribute__((ext_vector_type(16))) __bf16 b16x16;
typedef __attribute__((ext_vector_type(8))) __bf16 b16x8;
typedef __attribute__((ext_vector_type(4))) float f32x4;
typedef __attribute__((ext_vector_type(8))) float f32x8;
typedef __attribute__((ext_vector_type(16))) float f32x16;
typedef __attribute__((ext_vector_type(4))) unsigned u32x4;
typedef __attribute__((ext_vector_type(2))) unsigned u32x2;
typedef __attribute__((address_space(3))) char* lds_ptr;
#define DI __device__ __forceinline__
#define SBAR() __builtin_amdgcn_sched_barrier(0)

constexpr int S_ = 2048, D_ = 576, F_ = 4608, NH = 32;
constexpr unsigned KV_CHUNK = 9472, KV_HEAD = 16 * KV_CHUNK;
constexpr unsigned WS_KV = 0;
constexpr unsigned WS_QM = WS_KV + NH * KV_HEAD + 4096;
constexpr unsigned WS_QL = WS_QM + NH * 64 * 1024;
constexpr unsigned WS_G  = WS_QL + NH * 2048 * 4;
constexpr unsigned WS_O  = WS_G + 2048 * 4;
constexpr unsigned WS_P  = WS_O + 128 * 18 * 1024;
constexpr unsigned WS_END = WS_P + 256 * 144 * 1024;
constexpr int PIECE = 1056, KC = 4 * PIECE, SLOT = 16 * PIECE, NSLOT = 5;
constexpr int L_H = NSLOT * SLOT;
constexpr int L_TOTAL = L_H + 73728;

DI u32x4 make_srd(const void* p, unsigned bytes) {
  const uint64_t a = (uint64_t)p; u32x4 r; r[0] = (unsigned)a; r[1] = (unsigned)(a >> 32) & 0xffffu; r[2] = bytes; r[3] = 0x20000u; return r; }
DI void dma16(u32x4 srd, unsigned voff, unsigned soff, unsigned ldsdst) {
  asm volatile("s_mov_b32 m0, %0\n\ts_nop 0\n\tbuffer_load_dwordx4 %1, %2, %3 offen lds" :: "s"(ldsdst), "v"(voff), "s"(srd), "s"(soff) : "memory"); }
typedef short v4i16_t __attribute__((ext_vector_type(4)));
DI s16x8 trfrag(lds_ptr p) {
  const v4i16_t lo = __builtin_amdgcn_ds_read_tr16_b64_v4i16((__attribute__((address_space(3))) v4i16_t*)p);
  const v4i16_t hi = __builtin_amdgcn_ds_read_tr16_b64_v4i16((__attribute__((address_space(3))) v4i16_t*)(p + 256));
  return __builtin_shufflevector(lo, hi, 0, 1, 2, 3, 4, 5, 6, 7); }
DI s16x8 ldsr128(lds_ptr p) { return *(const __attribute__((address_space(3))) s16x8*)p; }
DI f32x4 mfma16(s16x8 a, s16x8 b, f32x4 c) { return __builtin_amdgcn_mfma_f32_16x16x32_bf16(__builtin_bit_cast(b16x8, a), __builtin_bit_cast(b16x8, b), c, 0, 0, 0); }
typedef __attribute__((ext_vector_type(2))) __bf16 b16x2;
typedef __attribute__((ext_vector_type(2))) float f32x2;
DI unsigned cvtpk(float lo, float hi) { f32x2 v = {lo, hi}; return __builtin_bit_cast(unsigned, __builtin_convertvector(v, b16x2)); }
#define WAIT_BAR(N) asm volatile("s_waitcnt vmcnt(" #N ") lgkmcnt(0)\n\ts_barrier" ::: "memory")
template <int N> DI void wait_bar() { asm volatile("s_waitcnt vmcnt(%0) lgkmcnt(0)\n\ts_barrier" :: "i"(N) : "memory"); }

struct Src { u32x4 srd; unsigned voff; };

__global__ __launch_bounds__(512, 2) void k_gemm(const char* __restrict__ x, const char* __restrict__ wqkv, const char* __restrict__ wg,
                                                 const char* __restrict__ wu, const char* __restrict__ wd, const char* __restrict__ gw, char* ws) {
  extern __shared__ __attribute__((aligned(16))) char smem[];
  const lds_ptr lds = (lds_ptr)smem; const unsigned lds0 = (unsigned)(uintptr_t)lds;
  const int b = blockIdx.x, xc = b & 7, rr_ = b >> 3, j = 2 * xc + (rr_ & 1), tg = rr_ >> 1;
  const int lane = threadIdx.x & 63, w = __builtin_amdgcn_readfirstlane(threadIdx.x >> 6), t = w & 3, hh = w >> 2;
  const int c = lane & 15, q = lane >> 4;
  const int rot = (tg + 4 * (j & 1)) % 9, rrot = tg & 3;
  const u32x4 srd_x = make_srd(x, S_ * D_ * 2), srd_qkv = make_srd(wqkv, D_ * 3 * D_ * 2), srd_w = make_srd(hh ? wu : wg, D_ * F_ * 2), srd_d = make_srd(wd, F_ * D_ * 2);
  const __amdgpu_buffer_rsrc_t rs_ws = __builtin_amdgcn_make_buffer_rsrc(ws, 0, WS_END, 0x20000);
  const int pi = lane >> 3, pc = lane & 7;
  const unsigned krow = 8 * (pi & 1) + 16 * (pi >> 2) + 4 * ((pi >> 1) & 1) + t;
  const unsigned frow = 16 * ((pi >> 1) & 1) + 4 * (pi & 1) + 8 * (pi >> 2) + t;
  const unsigned vo_qkv = krow * 3456 + pc * 16, vo_w = krow * 9216 + pc * 16, vo_d = frow * 1152 + pc * 16;
  const unsigned xrow = 8 * w + pi, vo_x = (128 * tg + xrow) * 1152 + ((pc ^ ((xrow >> 1) & 7)) * 16);
  const int colbase = j < 13 ? 128 * j : 1600;
  const int fullline = 9 * xc + ((j & 1) ? 5 : 0), halfline = 9 * xc + 4;
  const unsigned dst_w = lds0 + w * PIECE;
  auto kslice = [&](int i) { int v = i + rot; return v >= 9 ? v - 9 : v; };
  auto issue_qkv = [&](int i, int so) { const unsigned s = kslice(i) * 64 * 3456 + colbase * 2 + hh * 128; dma16(srd_qkv, vo_qkv, s, dst_w + so); dma16(srd_qkv, vo_qkv, s + 32 * 3456, dst_w + so + 8 * PIECE); };
  auto issue_x = [&](int i) { const unsigned s = kslice(i) * 128, d = lds0 + L_H + (i & 3) * 16384 + w * 1024; dma16(srd_x, vo_x, s, d); dma16(srd_x, vo_x, s + 64 * 1152, d + 8192); };
  auto issue_w = [&](int line, int i, int so) { const unsigned s = kslice(i) * 64 * 9216 + line * 128; dma16(srd_w, vo_w, s, dst_w + so); dma16(srd_w, vo_w, s + 32 * 9216, dst_w + so + 8 * PIECE); };
  auto dsoff = [&](int n) { const int i = (n * 57) >> 9, kp = n - 9 * i; int ds = i + rot; ds = ds >= 9 ? ds - 9 : ds;
    const int fr = 64 * fullline + 32 * ((kp == 8 && (j & 1)) ? -1 : kp); return n < 81 ? (unsigned)(fr * 1152 + ds * 128) : 0x7f000000u; };
  auto issue_d = [&](int s, int so) { dma16(srd_d, vo_d, dsoff(4 * s + hh), dst_w + so); dma16(srd_d, vo_d, dsoff(4 * s + 2 + hh), dst_w + so + 8 * PIECE); };
  const int trb = ((lane >> 2) & 3) * PIECE + ((q & 1) + 4 * (q >> 1)) * 128 + (lane & 3) * 8;
  int so = 0;
  auto nextso = [&](int v) { return v == (NSLOT - 1) * SLOT ? 0 : v + SLOT; };

  issue_qkv(0, 0); issue_x(0); issue_qkv(1, SLOT); issue_x(1); issue_qkv(2, 2 * SLOT); issue_x(2); issue_qkv(3, 3 * SLOT); issue_x(3); issue_qkv(4, 4 * SLOT);
  s16x8 xf[18][2];
  f32x4 acc[2][2][2];
  s16x8 X0, X1, Y0, Y1;
  const f32x4 z4 = {0.f, 0.f, 0.f, 0.f};
  wait_bar<14>();
  const bool vori = j >= 9;
  {
    const int xrb = L_H + (32 * t + c) * 128, xsw = (c >> 1) & 7;
#define RDX(i) do { _Pragma("unroll") for (int k2 = 0; k2 < 2; ++k2) _Pragma("unroll") for (int ts = 0; ts < 2; ++ts) \
      xf[2 * (i) + k2][ts] = ldsr128(lds + xrb + ((i) & 3) * 16384 + ts * 2048 + (((4 * k2 + q) ^ xsw) * 16)); } while (0)
#define MM(L, A0, A1, ks, FIRST) do { if (VORI) { \
      acc[L][0][0] = mfma16(xf[ks][0], A0, FIRST ? z4 : acc[L][0][0]); acc[L][0][1] = mfma16(xf[ks][1], A0, FIRST ? z4 : acc[L][0][1]); \
      acc[L][1][0] = mfma16(xf[ks][0], A1, FIRST ? z4 : acc[L][1][0]); acc[L][1][1] = mfma16(xf[ks][1], A1, FIRST ? z4 : acc[L][1][1]); } else { \
      acc[L][0][0] = mfma16(A0, xf[ks][0], FIRST ? z4 : acc[L][0][0]); acc[L][0][1] = mfma16(A0, xf[ks][1], FIRST ? z4 : acc[L][0][1]); \
      acc[L][1][0] = mfma16(A1, xf[ks][0], FIRST ? z4 : acc[L][1][0]); acc[L][1][1] = mfma16(A1, xf[ks][1], FIRST ? z4 : acc[L][1][1]); } } while (0)
#define QKV_ROUND(VORI_) do { constexpr bool VORI = VORI_; \
      lds_ptr p = lds + trb + hh * 64 + so; RDX(0); X0 = trfrag(p); X1 = trfrag(p + 32); \
      _Pragma("unroll") for (int i = 0; i < 9; ++i) { \
        Y0 = trfrag(p + KC); Y1 = trfrag(p + KC + 32); SBAR(); MM(0, X0, X1, 2 * i, (i == 0)); SBAR(); \
        X0 = trfrag(p + 2 * KC); X1 = trfrag(p + 2 * KC + 32); SBAR(); MM(1, Y0, Y1, 2 * i, (i == 0)); SBAR(); \
        Y0 = trfrag(p + 3 * KC); Y1 = trfrag(p + 3 * KC + 32); SBAR(); MM(0, X0, X1, 2 * i + 1, false); SBAR(); \
        if (i <= 5) wait_bar<10>(); else if (i == 6) wait_bar<8>(); else wait_bar<6>(); \
        const int fo = so; so = nextso(so); p = lds + trb + hh * 64 + so; \
        if (i < 8) RDX(i + 1); \
        X0 = trfrag(p); X1 = trfrag(p + 32); \
        if (i + 4 <= 8) issue_x(i + 4); \
        if (i + 5 <= 8) issue_qkv(i + 5, fo); else issue_w(fullline + rrot, i - 4, fo); \
        SBAR(); MM(1, Y0, Y1, 2 * i + 1, false); SBAR(); \
      } } while (0)
    if (vori) QKV_ROUND(true); else QKV_ROUND(false);
    if (j < 14) {
#pragma unroll
      for (int L = 0; L < 2; ++L)
#pragma unroll
        for (int u = 0; u < 2; ++u) {
          const int col0 = colbase + 64 * L + 32 * hh + 16 * u;
          if (j == 13 && L == 0) continue;
          if (!vori) {
            const int col = col0 + 4 * q, isk = col >= 576, cw = col - 576 * isk, cc = cw >> 2, pr = (cc * 57) >> 9 , pr2 = (cc >= 128) ? (cc >= 135 ? 15 : 14) : pr, cp = cc - 9 * pr2;
            const float sc = isk ? 1.0f : 0.23570226039551584f * 1.4426950408889634f;
#pragma unroll
            for (int ts = 0; ts < 2; ++ts) {
              const int r32 = 16 * ts + c; unsigned a0, a1;
              if (cp == 4) {
                if (isk) { a0 = WS_KV + (2 * pr2) * KV_HEAD + tg * KV_CHUNK + 4096 + r32 * 16 + t * 4; a1 = a0 + KV_HEAD; }
                else     { a0 = WS_QL + ((2 * pr2) * 2048 + 128 * tg + 32 * t + r32) * 4; a1 = a0 + 2048 * 4; }
              } else {
                const int head = 2 * pr2 + (cp > 4), cm = cp > 4 ? cp - 5 : cp, in = (cm >> 1) * 512 + r32 * 16 + (cm & 1) * 8;
                a0 = isk ? WS_KV + head * KV_HEAD + tg * KV_CHUNK + t * 1024 + in : WS_QM + (head * 64 + 4 * tg + t) * 1024 + in; a1 = a0 + 4;
              }
              const f32x4 v = acc[L][u][ts];
              __builtin_amdgcn_raw_buffer_store_b32(cvtpk(v[0] * sc, v[1] * sc), rs_ws, a0, 0, 0);
              __builtin_amdgcn_raw_buffer_store_b32(cvtpk(v[2] * sc, v[3] * sc), rs_ws, a1, 0, 0);
            }
          } else {
            const int cw = col0 + c - 1152, head = (cw * 3641) >> 16, d = cw - 18 * head;
            const int pos = d < 4 ? d : d < 12 ? d + 5 : d < 16 ? d - 8 : d + 1;
#pragma unroll
            for (int ts = 0; ts < 2; ++ts) {
              const unsigned a = WS_KV + head * KV_HEAD + tg * KV_CHUNK + 4608 + t * 1216 + ts * 608 + (q >> 1) * 304 + pos * 16 + (q & 1) * 8;
              const f32x4 v = acc[L][u][ts]; u32x2 o = {cvtpk(v[0], v[1]), cvtpk(v[2], v[3])};
              __builtin_amdgcn_raw_buffer_store_b64(o, rs_ws, a, 0, 0);
            }
          }
        }
    } else if (j == 14) {
      const int id = threadIdx.x, head = id >> 4, e = id & 15;
      const u32x4 ones = {0x3f803f80u, 0x3f803f80u, 0x3f803f80u, 0x3f803f80u};
      __builtin_amdgcn_raw_buffer_store_b128(ones, rs_ws, WS_KV + head * KV_HEAD + tg * KV_CHUNK + 4608 + e * 304 + 128, 0, 0);
    } else if (hh == 0) {
      f32x4 g0 = z4, g1 = z4;
#pragma unroll
      for (int ks = 0; ks < 18; ++ks) {
        const s16x8 gf = *(const s16x8*)(gw + (kslice(ks >> 1) * 64 + (ks & 1) * 32 + 8 * q) * 2);
        g0 = mfma16(gf, xf[ks][0], g0); g1 = mfma16(gf, xf[ks][1], g1);
      }
      const float lg = q == 0 ? g0[0] : g1[0];
      const float g = __builtin_amdgcn_rcpf(1.0f + __builtin_amdgcn_exp2f(-1.4426950408889634f * lg));
      if (q < 2) __builtin_amdgcn_raw_buffer_store_b32(__float_as_uint(g), rs_ws, WS_G + (128 * tg + 32 * t + 16 * q + c) * 4, 0, 0);
    }
  }
  constexpr bool VORI = false;
  float za[16], aa[16];
  const float K1 = -2.0f * 0.7978845608028654f * 1.4426950408889634f, K2 = K1 * 0.044715f;
#define GELU_A(n) do { _Pragma("unroll") for (int e = 0; e < (n); ++e) { const float gv = acc[0][e >> 3][(e >> 2) & 1][e & 3], uv = acc[1][e >> 3][(e >> 2) & 1][e & 3]; \
      za[e] = gv * __builtin_fmaf(gv * gv, K2, K1); aa[e] = gv * uv; } } while (0)
#define GELU_B(e) do { aa[e] = aa[e] * __builtin_amdgcn_rcpf(1.0f + __builtin_amdgcn_exp2f(za[e])); asm volatile("" : "+v"(aa[e])); } while (0)
#define H_WRITE(kap) do { _Pragma("unroll") for (int ts = 0; ts < 2; ++ts) { u32x4 hv = {cvtpk(aa[4 * ts], aa[4 * ts + 1]), cvtpk(aa[4 * ts + 2], aa[4 * ts + 3]), cvtpk(aa[8 + 4 * ts], aa[9 + 4 * ts]), cvtpk(aa[10 + 4 * ts], aa[11 + 4 * ts])}; \
      *(__attribute__((address_space(3))) u32x4*)(lds + L_H + ((kap) * 8 + 2 * t + ts) * 1024 + lane * 16) = hv; } } while (0)
#pragma unroll
  for (int e = 0; e < 16; ++e) { za[e] = 0.f; aa[e] = 0.f; }
  lds_ptr p = lds + trb + hh * 64 + so;
  for (int r = 0; r < 4; ++r) {
    const int line = fullline + ((r + rrot) & 3), nline = r < 3 ? fullline + ((r + 1 + rrot) & 3) : halfline;
    const int pk = 2 * ((r - 1 + rrot) & 3) + hh;
    const int nb = r < 3 ? hh * 64 : ((j & 1) * 2 + hh) * 32;
#pragma unroll
    for (int i = 0; i < 9; ++i) {
      Y0 = trfrag(p + KC); Y1 = trfrag(p + KC + 32); SBAR(); MM(0, X0, X1, 2 * i, (i == 0)); if (i < 8) GELU_B(2 * i); SBAR();
      X0 = trfrag(p + 2 * KC); X1 = trfrag(p + 2 * KC + 32); SBAR(); MM(1, Y0, Y1, 2 * i, (i == 0)); if (i < 8) GELU_B(2 * i + 1); else if (r > 0) H_WRITE(pk); SBAR();
      Y0 = trfrag(p + 3 * KC); Y1 = trfrag(p + 3 * KC + 32); SBAR(); MM(0, X0, X1, 2 * i + 1, false); SBAR();
      wait_bar<6>();
      const int fo = so; so = nextso(so); p = lds + trb + (i == 8 ? nb : hh * 64) + so;
      X0 = trfrag(p); X1 = trfrag(p + 32);
      if (i + 5 <= 8) issue_w(line, i + 5, fo); else issue_w(nline, i - 4, fo);
      SBAR(); MM(1, Y0, Y1, 2 * i + 1, false); SBAR();
    }
    GELU_A(16);
  }
  {
    const int pk = 2 * ((3 + rrot) & 3) + hh, nb = ((j & 1) * 2 + hh) * 32;
#define MH(L, A0, ks, FIRST) do { acc[L][0][0] = mfma16(A0, xf[ks][0], FIRST ? z4 : acc[L][0][0]); acc[L][0][1] = mfma16(A0, xf[ks][1], FIRST ? z4 : acc[L][0][1]); } while (0)
#pragma unroll
    for (int i = 0; i < 9; ++i) {
      Y0 = trfrag(p + KC); X1 = trfrag(p + 2 * KC); Y1 = trfrag(p + 3 * KC); SBAR(); MH(0, X0, 2 * i, (i == 0)); if (i < 8) GELU_B(2 * i); SBAR();
      MH(1, Y0, 2 * i, (i == 0)); if (i < 8) GELU_B(2 * i + 1); else H_WRITE(pk); SBAR();
      wait_bar<6>();
      const int fo = so; so = nextso(so); p = lds + trb + (i == 8 ? t * 32 : nb) + so;
      X0 = trfrag(p);
      if (i + 5 <= 8) issue_w(halfline, i + 5, fo); else issue_d(i - 4, fo);
      SBAR(); MH(0, X1, 2 * i + 1, false); MH(1, Y1, 2 * i + 1, false); SBAR();
    }
    GELU_A(8);
#pragma unroll
    for (int e = 0; e < 8; ++e) GELU_B(e);
#pragma unroll
    for (int ts = 0; ts < 2; ++ts) { u32x2 hv = {cvtpk(aa[4 * ts], aa[4 * ts + 1]), cvtpk(aa[4 * ts + 2], aa[4 * ts + 3])};
      *(__attribute__((address_space(3))) u32x2*)(lds + L_H + (64 + 2 * t + ts) * 1024 + lane * 16 + hh * 8) = hv; }
  }
  asm volatile("s_waitcnt lgkmcnt(0)\n\ts_barrier" ::: "memory");
  {
    s16x8 hf[9][4];
#pragma unroll
    for (int kp = 0; kp < 9; ++kp)
#pragma unroll
      for (int ts = 0; ts < 4; ++ts) hf[kp][ts] = ldsr128(lds + L_H + (kp * 8 + 4 * hh + ts) * 1024 + lane * 16);
    f32x4 dc[4];
    const unsigned pbase = WS_P + (tg * 16 + j) * 144 * 1024 + (t * 4 + 2 * hh) * 1024 + lane * 16;
#pragma unroll
    for (int n = 0; n < 81; ++n) {
      const int kp = n % 9, i = n / 9;
      s16x8 nx;
      if ((n & 3) == 3) {
        wait_bar<6>();
        const int fo = so; so = nextso(so); p = lds + trb + t * 32 + so; nx = trfrag(p);
        issue_d((n >> 2) + 5, fo);
      } else if (n < 80) nx = trfrag(p + ((n & 3) + 1) * KC);
      SBAR();
#pragma unroll
      for (int ts = 0; ts < 4; ++ts) dc[ts] = mfma16(X0, hf[kp][ts], kp == 0 ? z4 : dc[ts]);
      SBAR();
      if (kp == 8) {
        int ds = i + rot; ds = ds >= 9 ? ds - 9 : ds;
#pragma unroll
        for (int tp = 0; tp < 2; ++tp) { u32x4 o = {cvtpk(dc[2 * tp][0], dc[2 * tp][1]), cvtpk(dc[2 * tp][2], dc[2 * tp][3]), cvtpk(dc[2 * tp + 1][0], dc[2 * tp + 1][1]), cvtpk(dc[2 * tp + 1][2], dc[2 * tp + 1][3])};
          __builtin_amdgcn_raw_buffer_store_b128(o, rs_ws, pbase + tp * 1024, ds * 16 * 1024, 0); }
      }
      if (n < 80) X0 = nx;
    }
    asm volatile("s_waitcnt vmcnt(0)" ::: "memory");
  }
}

DI f32x16 mfma32(s16x8 a, s16x8 b, f32x16 c) { return __builtin_amdgcn_mfma_f32_32x32x16_bf16(__builtin_bit_cast(b16x8, a), __builtin_bit_cast(b16x8, b), c, 0, 0, 0); }
DI f32x16 mfma32k8(u32x2 a, u32x2 b, f32x16 c) { return __builtin_amdgcn_mfma_f32_32x32x8bf16_1k(__builtin_bit_cast(s16x4, a), __builtin_bit_cast(s16x4, b), c, 0, 0, 0); }
DI f32x16 pv32(s16x8 a, f32x8 p, f32x16 c) { return __builtin_amdgcn_smfmac_f32_32x32x32_bf16(__builtin_bit_cast(b16x8, a), __builtin_bit_cast(b16x16, p), c, 0xDDDD, 0, 0); }
#define LO8(v) __builtin_shufflevector(v, v, 0, 1, 2, 3, 4, 5, 6, 7)
#define HI8(v) __builtin_shufflevector(v, v, 8, 9, 10, 11, 12, 13, 14, 15)
DI float rowmax16(const f32x16& s) {
  float a = __builtin_fmaxf(__builtin_fmaxf(s[0], s[1]), s[2]);
#pragma unroll
  for (int r = 3; r < 15; r += 2) a = __builtin_fmaxf(__builtin_fmaxf(a, s[r]), s[r + 1]);
  return __builtin_fmaxf(a, s[15]); }
DI float xhalf_max(float m) { auto rr = __builtin_amdgcn_permlane32_swap(__float_as_uint(m), __float_as_uint(m), false, false); return __builtin_fmaxf(__uint_as_float(rr[0]), __uint_as_float(rr[1])); }

__global__ __launch_bounds__(512, 2) void k_attn(char* ws) {
  extern __shared__ __attribute__((aligned(16))) char smem[];
  const lds_ptr lds = (lds_ptr)smem; const unsigned lds0 = (unsigned)(uintptr_t)lds;
  const int b = blockIdx.x, xc = b & 7, rr_ = b >> 3, head = 4 * xc + (rr_ & 3), qb = rr_ >> 2;
  const int lane = threadIdx.x & 63, w = __builtin_amdgcn_readfirstlane(threadIdx.x >> 6), r = lane & 31, hl = lane >> 5;
  const u32x4 srd_kv = make_srd(ws + WS_KV + head * KV_HEAD, KV_HEAD);
  const __amdgpu_buffer_rsrc_t rs_ws = __builtin_amdgcn_make_buffer_rsrc(ws, 0, WS_END, 0x20000);
  const int tile = 8 * qb + w;
  const s16x8 qm = __builtin_bit_cast(s16x8, __builtin_amdgcn_raw_buffer_load_b128(rs_ws, WS_QM + (head * 64 + tile) * 1024 + lane * 16, 0, 0));
  unsigned ql = __builtin_amdgcn_raw_buffer_load_b32(rs_ws, WS_QL + (head * 2048 + 32 * tile + r) * 4, 0, 0);
#pragma unroll
  for (int i = 0; i < 19; ++i) dma16(srd_kv, lane * 16, (8 * i + w) * 1024, lds0 + (8 * i + w) * 1024);
  ql = hl ? 0u : ql;
  const u32x2 qa = {ql, 0u}, qbb = {0u, ql};
  const int pos = r < 4 ? r : r < 12 ? r + 5 : r < 16 ? r - 8 : r < 18 ? r + 1 : 8;
  const lds_ptr kp0 = lds + lane * 16, lp0 = lds + 4096 + r * 16, vp0 = lds + 4608 + hl * 304 + pos * 16;
  wait_bar<17>();
  f32x16 negm;
  {
    const f32x16 z = {}; const u32x4 kl = *(const __attribute__((address_space(3))) u32x4*)lp0;
    f32x16 s = mfma32(ldsr128(kp0), qm, z); s = mfma32k8((u32x2){kl[0], kl[1]}, qa, s);
    const float m = xhalf_max(rowmax16(s));
#pragma unroll
    for (int e = 0; e < 16; ++e) negm[e] = -m;
  }
  f32x16 o;
  for (int pass = 0;; ++pass) {
    asm volatile("" : "+v"(negm));
    o = (f32x16){};
    f32x16 sa, sb; u32x4 kl = *(const __attribute__((address_space(3))) u32x4*)lp0;
    sa = mfma32(ldsr128(kp0), qm, negm); sa = mfma32k8((u32x2){kl[0], kl[1]}, qa, sa);
    s16x8 kf = ldsr128(kp0 + 1024), v0 = ldsr128(vp0), v1 = ldsr128(vp0 + 608);
#define EXP4(S, i) do { S[i] = __builtin_amdgcn_exp2f(S[i]); S[i + 1] = __builtin_amdgcn_exp2f(S[i + 1]); S[i + 2] = __builtin_amdgcn_exp2f(S[i + 2]); S[i + 3] = __builtin_amdgcn_exp2f(S[i + 3]); } while (0)
#define STEP(CUR, NXT, KL, QV, KN, VN) do { \
      NXT = mfma32(kf, qm, negm); kf = ldsr128(KN); EXP4(CUR, 0); SBAR(); \
      NXT = mfma32k8(KL, QV, NXT); EXP4(CUR, 4); EXP4(CUR, 8); SBAR(); \
      o = pv32(v0, LO8(CUR), o); v0 = ldsr128(VN); EXP4(CUR, 12); SBAR(); \
      o = pv32(v1, HI8(CUR), o); v1 = ldsr128((VN) + 608); SBAR(); } while (0)
    for (int ch = 0; ch < 16; ++ch) {
      if (pass == 0) { if (ch == 0) wait_bar<14>(); else if (ch == 3) wait_bar<9>(); else if (ch == 7) wait_bar<0>(); }
      const int co = ch * KV_CHUNK, cn = (ch < 15 ? co + KV_CHUNK : 0);
      const u32x4 kl2 = *(const __attribute__((address_space(3))) u32x4*)(lp0 + cn);
      STEP(sa, sb, ((u32x2){kl[0], kl[1]}), qbb, kp0 + co + 2048, vp0 + co + 1216);
      STEP(sb, sa, ((u32x2){kl[2], kl[3]}), qa, kp0 + co + 3072, vp0 + co + 2432);
      STEP(sa, sb, ((u32x2){kl[2], kl[3]}), qbb, kp0 + cn, vp0 + co + 3648);
      STEP(sb, sa, ((u32x2){kl2[0], kl2[1]}), qa, kp0 + cn + 1024, vp0 + cn);
      kl = kl2;
    }
    float chk = __builtin_fabsf(o[12]);
#pragma unroll
    for (int e = 0; e < 10; ++e) chk = __builtin_fmaxf(chk, __builtin_fabsf(o[e]) * 0x1p-60f);
    bool bad = !(o[12] < 0x1p60f);
#pragma unroll
    for (int e = 0; e < 10; ++e) bad |= !(__builtin_fabsf(o[e]) < 0x1p120f);
    if (pass == 1 || !__any(bad)) break;
    float m = -INFINITY; const f32x16 z = {};
    for (int tl = 0; tl < 64; ++tl) {
      const int co = (tl >> 2) * KV_CHUNK; const unsigned k1 = *(const __attribute__((address_space(3))) unsigned*)(lp0 + co + (tl & 3) * 4);
      f32x16 s = mfma32(ldsr128(kp0 + co + (tl & 3) * 1024), qm, z); s = mfma32k8((u32x2){k1, 0u}, qa, s);
      m = __builtin_fmaxf(m, rowmax16(s));
    }
    m = xhalf_max(m);
#pragma unroll
    for (int e = 0; e < 16; ++e) negm[e] = -m;
  }
  const float rl = 1.0f / o[12];
  const int tok = 32 * tile + r;
  const u32x4 om = {cvtpk(o[0] * rl, o[1] * rl), cvtpk(o[2] * rl, o[3] * rl), cvtpk(o[4] * rl, o[5] * rl), cvtpk(o[6] * rl, o[7] * rl)};
  const unsigned ob = WS_O + (tok >> 4) * 18 * 1024 + (tok & 15) * 16;
  __builtin_amdgcn_raw_buffer_store_b128(om, rs_ws, ob + (head >> 1) * 1024 + (2 * (head & 1) + hl) * 256, 0, 0);
  if (hl == 0) __builtin_amdgcn_raw_buffer_store_b32(cvtpk(o[8] * rl, o[9] * rl), rs_ws, ob + (16 + (head >> 4)) * 1024 + ((head >> 2) & 3) * 256 + (head & 3) * 4, 0, 0);
}

__global__ __launch_bounds__(512, 2) void k_final(const char* __restrict__ wo, char* ws, char* out) {
  extern __shared__ __attribute__((aligned(16))) char smem[];
  const lds_ptr lds = (lds_ptr)smem; const unsigned lds0 = (unsigned)(uintptr_t)lds;
  const int b = blockIdx.x, ds = b % 9, th = b / 9;
  const int lane = threadIdx.x & 63, w = __builtin_amdgcn_readfirstlane(threadIdx.x >> 6), t = w & 3, hh = w >> 2, c = lane & 15, q = lane >> 4;
  const u32x4 srd_wo = make_srd(wo, D_ * D_ * 2);
  const __amdgpu_buffer_rsrc_t rs_ws = __builtin_amdgcn_make_buffer_rsrc(ws, 0, WS_END, 0x20000);
  const __amdgpu_buffer_rsrc_t rs_out = __builtin_amdgcn_make_buffer_rsrc(out, 0, S_ * D_ * 2, 0x20000);
  const int pi = lane >> 3, pc = lane & 7;
  const int qq = (pi & 1) + 2 * (pi >> 2), jj = t + 4 * ((pi >> 1) & 1);
#pragma unroll
  for (int i = 0; i < 9; ++i) {
    const int kap = 2 * i + hh;
    const int rowm = 18 * (2 * kap + (qq >> 1)) + 4 * (qq & 1) + (jj & 3) + 8 * (jj >> 2);
    const int rowl = 18 * (16 * (kap - 16) + 4 * qq + (jj >> 1)) + 16 + (jj & 1);
    dma16(srd_wo, (kap < 16 ? rowm : rowl) * 1152 + pc * 16, ds * 128, lds0 + kap * KC + t * PIECE);
  }
  s16x8 of[18][2];
#pragma unroll
  for (int kap = 0; kap < 18; ++kap)
#pragma unroll
    for (int ts = 0; ts < 2; ++ts) of[kap][ts] = __builtin_bit_cast(s16x8, __builtin_amdgcn_raw_buffer_load_b128(rs_ws, WS_O + ((4 * th + 2 * hh + ts) * 18 + kap) * 1024 + lane * 16, 0, 0));
  const int tg = th >> 1, tp = 2 * (th & 1) + hh;
  float bs[8];
#pragma unroll
  for (int e = 0; e < 8; ++e) bs[e] = 0.f;
#pragma unroll
  for (int jx = 0; jx < 16; ++jx) {
    const u32x4 pv = __builtin_amdgcn_raw_buffer_load_b128(rs_ws, WS_P + ((tg * 16 + jx) * 144 + (4 * ds + t) * 4 + tp) * 1024 + lane * 16, 0, 0);
#pragma unroll
    for (int e = 0; e < 4; ++e) { bs[2 * e] += __uint_as_float(pv[e] << 16); bs[2 * e + 1] += __uint_as_float(pv[e] & 0xffff0000u); }
  }
  float g[2];
#pragma unroll
  for (int ts = 0; ts < 2; ++ts) g[ts] = __uint_as_float(__builtin_amdgcn_raw_buffer_load_b32(rs_ws, WS_G + (64 * th + 32 * hh + 16 * ts + c) * 4, 0, 0));
  WAIT_BAR(0);
  const lds_ptr p = lds + ((lane >> 2) & 3) * PIECE + ((q & 1) + 4 * (q >> 1)) * 128 + (lane & 3) * 8 + t * 32;
  f32x4 a0 = {0.f, 0.f, 0.f, 0.f}, a1 = a0;
#pragma unroll
  for (int kap = 0; kap < 18; ++kap) { const s16x8 A = trfrag(p + kap * KC); a0 = mfma16(A, of[kap][0], a0); a1 = mfma16(A, of[kap][1], a1); }
#pragma unroll
  for (int ts = 0; ts < 2; ++ts) { const f32x4 a = ts ? a1 : a0; float v[4];
#pragma unroll
    for (int e = 0; e < 4; ++e) v[e] = g[ts] * a[e] + (1.0f - g[ts]) * bs[4 * ts + e];
    u32x2 o = {cvtpk(v[0], v[1]), cvtpk(v[2], v[3])};
    __builtin_amdgcn_raw_buffer_store_b64(o, rs_out, ((64 * th + 32 * hh + 16 * ts + c) * 576 + 64 * ds + 16 * t + 4 * q) * 2, 0, 0); }
}

extern "C" void kernel_launch(void* const* d_in, const int* in_sizes, int n_in, void* d_out, int out_size, void* d_ws, size_t ws_size, hipStream_t stream) {
  (void)hipFuncSetAttribute((const void*)k_gemm, hipFuncAttributeMaxDynamicSharedMemorySize, L_TOTAL);
  (void)hipFuncSetAttribute((const void*)k_attn, hipFuncAttributeMaxDynamicSharedMemorySize, 152 * 1024);
  (void)hipFuncSetAttribute((const void*)k_final, hipFuncAttributeMaxDynamicSharedMemorySize, 18 * KC);
  k_gemm<<<256, 512, L_TOTAL, stream>>>((const char*)d_in[0], (const char*)d_in[1], (const char*)d_in[4], (const char*)d_in[5], (const char*)d_in[6], (const char*)d_in[7], (char*)d_ws);
  k_attn<<<256, 512, 152 * 1024, stream>>>((char*)d_ws);
  k_final<<<288, 512, 18 * KC, stream>>>((const char*)d_in[2], (char*)d_ws, (char*)d_out);
}
```

```cpp
#include <hip/hip_runtime.h>
#include <hip/hip_bf16.h>
#include <stdint.h>
#include <type_traits>

typedef __attribute__((ext_vector_type(8))) short s16x8;
typedef __attribute__((ext_vector_type(4))) short s16x4;
typedef __attribute__((ext_vector_type(8))) __bf16 b16x8;
typedef __attribute__((ext_vector_type(2))) __bf16 b16x2;
typedef __attribute__((ext_vector_type(2))) float f32x2;
typedef __attribute__((ext_vector_type(4))) float f32x4;
typedef __attribute__((ext_vector_type(16))) float f32x16;
typedef __attribute__((ext_vector_type(4))) unsigned u32x4;
typedef __attribute__((ext_vector_type(2))) unsigned u32x2;
typedef __attribute__((address_space(3))) char* lds_ptr;
#define LAS __attribute__((address_space(3)))
#define DI __device__ __forceinline__
#define SBAR() __builtin_amdgcn_sched_barrier(0)
#define PIN(x) asm volatile("" : "+v"(x))

constexpr int S_ = 2048, D_ = 576, F_ = 4608;
constexpr unsigned WS_CTL = 0;
constexpr unsigned WS_FC = 4096;
constexpr unsigned WS_G  = WS_FC + 32768;
constexpr unsigned WS_QK = WS_G + 8192;
constexpr unsigned WS_V  = WS_QK + 288 * 16384;
constexpr unsigned WS_WO = WS_V + 32 * 73728 + 32768;
constexpr unsigned WS_O  = WS_WO + 36 * 18 * 1024;
constexpr unsigned WS_P  = WS_O + 64 * 36 * 1024;
constexpr unsigned WS_END = WS_P + 256 * 144 * 1024;
constexpr int PIECE = 1056, KC = 4 * PIECE, SLOT = 16 * PIECE, NSLOT = 5;
constexpr int L_H = NSLOT * SLOT;
constexpr int L_GW = L_H + 73728;
constexpr int L_K0 = L_H, L_V0 = L_H + 40960, L_K1 = 0, L_V1 = 40960;
constexpr int L_C = 78592, L_FL = 163328;
constexpr int L_TOTAL = 163840;
constexpr unsigned OOB = 0x7f000000u;
constexpr int SC1 = 16;


DI u32x4 make_srd(const void* p, unsigned bytes) {
  const uint64_t a = (uint64_t)p; u32x4 r; r[0] = (unsigned)a; r[1] = (unsigned)(a >> 32) & 0xffffu; r[2] = bytes; r[3] = 0x20000u; return r; }
DI void dma16(u32x4 srd, unsigned voff, unsigned soff, unsigned ldsdst) {
  asm volatile("s_mov_b32 m0, %0\n\ts_nop 0\n\tbuffer_load_dwordx4 %1, %2, %3 offen lds" :: "s"(ldsdst), "v"(voff), "s"(srd), "s"(soff) : "memory"); }
DI void dma16i(u32x4 srd, unsigned voff, unsigned sbase, unsigned simm, unsigned dbase, unsigned dimm) {
  unsigned tmp; asm volatile("s_add_u32 m0, %1, %2\n\ts_add_u32 %0, %3, %4\n\tbuffer_load_dwordx4 %5, %6, %0 offen lds" : "=&s"(tmp) : "s"(dbase), "i"(dimm), "s"(sbase), "i"(simm), "v"(voff), "s"(srd) : "memory", "scc"); }
DI void dma16ic(u32x4 srd, unsigned voff, unsigned sbase, unsigned simm, unsigned dbase, unsigned dimm) {
  unsigned tmp; asm volatile("s_add_u32 m0, %1, %2\n\ts_add_u32 %0, %3, %4\n\tbuffer_load_dwordx4 %5, %6, %0 offen sc1 lds" : "=&s"(tmp) : "s"(dbase), "i"(dimm), "s"(sbase), "i"(simm), "v"(voff), "s"(srd) : "memory", "scc"); }
typedef short v4i16_t __attribute__((ext_vector_type(4)));
DI s16x8 trfrag(lds_ptr p) {
  const v4i16_t lo = __builtin_amdgcn_ds_read_tr16_b64_v4i16((LAS v4i16_t*)p);
  const v4i16_t hi = __builtin_amdgcn_ds_read_tr16_b64_v4i16((LAS v4i16_t*)(p + 256));
  return __builtin_shufflevector(lo, hi, 0, 1, 2, 3, 4, 5, 6, 7); }
DI s16x8 ldsr128(lds_ptr p) { return *(const LAS s16x8*)p; }
DI f32x4 mfma16(s16x8 a, s16x8 b, f32x4 c) { return __builtin_amdgcn_mfma_f32_16x16x32_bf16(__builtin_bit_cast(b16x8, a), __builtin_bit_cast(b16x8, b), c, 0, 0, 0); }
DI unsigned cvtpk(float lo, float hi) { f32x2 v = {lo, hi}; return __builtin_bit_cast(unsigned, __builtin_convertvector(v, b16x2)); }
DI float fmul1(float a, float b) { float r; asm("v_mul_f32 %0, %1, %2" : "=v"(r) : "v"(a), "v"(b)); return r; }
#define MF(c, a_, b_) asm volatile("v_mfma_f32_16x16x32_bf16 %0, %1, %2, %0" : "+v"(c) : "v"(a_), "v"(b_))
#define MF0(c, a_, b_) asm volatile("v_mfma_f32_16x16x32_bf16 %0, %1, %2, 0" : "=&v"(c) : "v"(a_), "v"(b_))
DI int fresh_lane() { int l; asm volatile("v_mbcnt_lo_u32_b32 %0, -1, 0\n\tv_mbcnt_hi_u32_b32 %0, -1, %0" : "=v"(l)); return l; }
template <int N> DI void vm_bar() { asm volatile("s_waitcnt vmcnt(%0)\n\ts_barrier" :: "i"(N) : "memory"); }
template <int N> DI void lgkm() { __builtin_amdgcn_s_waitcnt(0xC07F | (N << 8)); }
template <int I, int N, typename F> DI void sfor(F&& f) { if constexpr (I < N) { f(std::integral_constant<int, I>{}); sfor<I + 1, N>(f); } }

DI f32x16 mfma32(s16x8 a, s16x8 b, f32x16 c) { return __builtin_amdgcn_mfma_f32_32x32x16_bf16(__builtin_bit_cast(b16x8, a), __builtin_bit_cast(b16x8, b), c, 0, 0, 0); }
DI f32x16 mfma32k8(u32x2 a, u32x2 b, f32x16 c) { return __builtin_amdgcn_mfma_f32_32x32x8bf16_1k(__builtin_bit_cast(s16x4, a), __builtin_bit_cast(s16x4, b), c, 0, 0, 0); }
DI float rowmax16(const f32x16& s) {
  float a = __builtin_fmaxf(__builtin_fmaxf(s[0], s[1]), s[2]);
#pragma unroll
  for (int r = 3; r < 15; r += 2) a = __builtin_fmaxf(__builtin_fmaxf(a, s[r]), s[r + 1]);
  return __builtin_fmaxf(a, s[15]); }
DI float xhalf_max(float m) { auto rr = __builtin_amdgcn_permlane32_swap(__float_as_uint(m), __float_as_uint(m), false, false); return __builtin_fmaxf(__uint_as_float(rr[0]), __uint_as_float(rr[1])); }

typedef __attribute__((ext_vector_type(32))) float f32x32;
typedef __attribute__((ext_vector_type(16))) unsigned u32x16;
typedef __attribute__((ext_vector_type(32))) unsigned u32x32;
typedef __attribute__((ext_vector_type(8))) unsigned u32x8;
#define ATT_SEG_ASM \
  "ds_read2_b64 v[222:225], v250 offset0:0 offset1:128\n\t" \
  "ds_read2st64_b32 v[230:231], v251 offset0:0 offset1:1\n\t" \
  "ds_read2_b64 v[226:229], v250 offset0:32 offset1:160\n\t" \
  "ds_read_b128 v[234:237], v252 offset:32\n\t" \
  "ds_read_b128 v[238:241], v252 offset:0\n\t" \
  "ds_read_b128 v[242:245], v252 offset:1152\n\t" \
  "v_mov_b32 v166, 0\n\t" \
  "v_mov_b32 v167, 0\n\t" \
  "v_mov_b32 v168, 0\n\t" \
  "v_mov_b32 v169, 0\n\t" \
  "v_mov_b32 v170, 0\n\t" \
  "v_mov_b32 v171, 0\n\t" \
  "v_mov_b32 v172, 0\n\t" \
  "v_mov_b32 v173, 0\n\t" \
  "v_mov_b32 v246, 0\n\t" \
  "v_mov_b32 v247, 0\n\t" \
  "v_mov_b32 v248, 0\n\t" \
  "v_mov_b32 v249, 0\n\t" \
  "s_waitcnt lgkmcnt(4)\n\t" \
  "v_mfma_f32_32x32x16_bf16 v[126:141], v[222:225], v[206:209], 0\n\t" \
  "v_mfma_f32_32x32x8_bf16 v[126:141], v[230:231], v[214:215], v[126:141]\n\t" \
  "s_waitcnt lgkmcnt(0)\n\t" \
  "s_nop 10\n\t" \
  "1:\n\t" \
  "v_mfma_f32_32x32x16_bf16 v[150:165], v[222:225], v[210:213], 0\n\t" \
  "v_exp_f32 v134, v134\n\t" \
  "v_exp_f32 v135, v135\n\t" \
  "v_exp_f32 v136, v136\n\t" \
  "v_exp_f32 v137, v137\n\t" \
  "v_mfma_f32_32x32x8_bf16 v[150:165], v[230:231], v[218:219], v[150:165]\n\t" \
  "v_exp_f32 v138, v138\n\t" \
  "v_exp_f32 v139, v139\n\t" \
  "v_exp_f32 v140, v140\n\t" \
  "v_exp_f32 v141, v141\n\t" \
  "v_smfmac_f32_32x32x32_bf16 v[190:205], v[246:249], v[166:173], v255\n\t" \
  "ds_read2st64_b32 v[232:233], v251 offset0:2 offset1:3\n\t" \
  "v_exp_f32 v126, v126\n\t" \
  "v_exp_f32 v127, v127\n\t" \
  "v_exp_f32 v128, v128\n\t" \
  "v_exp_f32 v129, v129\n\t" \
  "v_smfmac_f32_32x32x32_bf16 v[174:189], v[234:237], v[134:141], v255\n\t" \
  "s_waitcnt lgkmcnt(1)\n\t" \
  "v_exp_f32 v130, v130\n\t" \
  "v_exp_f32 v131, v131\n\t" \
  "v_exp_f32 v132, v132\n\t" \
  "v_exp_f32 v133, v133\n\t" \
  "v_mfma_f32_32x32x16_bf16 v[134:149], v[226:229], v[206:209], 0\n\t" \
  "ds_read2_b64 v[222:225], v250 offset0:64 offset1:192\n\t" \
  "v_exp_f32 v158, v158\n\t" \
  "v_exp_f32 v159, v159\n\t" \
  "v_exp_f32 v160, v160\n\t" \
  "v_exp_f32 v161, v161\n\t" \
  "v_mfma_f32_32x32x8_bf16 v[134:149], v[230:231], v[216:217], v[134:149]\n\t" \
  "v_exp_f32 v162, v162\n\t" \
  "v_exp_f32 v163, v163\n\t" \
  "v_exp_f32 v164, v164\n\t" \
  "v_exp_f32 v165, v165\n\t" \
  "v_smfmac_f32_32x32x32_bf16 v[174:189], v[238:241], v[126:133], v255\n\t" \
  "ds_read_b128 v[246:249], v252 offset:1184\n\t" \
  "v_exp_f32 v150, v150\n\t" \
  "v_exp_f32 v151, v151\n\t" \
  "v_exp_f32 v152, v152\n\t" \
  "v_exp_f32 v153, v153\n\t" \
  "v_smfmac_f32_32x32x32_bf16 v[190:205], v[234:237], v[158:165], v255\n\t" \
  "ds_read_b128 v[234:237], v252 offset:2336\n\t" \
  "v_exp_f32 v154, v154\n\t" \
  "v_exp_f32 v155, v155\n\t" \
  "v_exp_f32 v156, v156\n\t" \
  "v_exp_f32 v157, v157\n\t" \
  "v_mfma_f32_32x32x16_bf16 v[158:173], v[226:229], v[210:213], 0\n\t" \
  "v_exp_f32 v134, v134\n\t" \
  "v_exp_f32 v135, v135\n\t" \
  "v_exp_f32 v136, v136\n\t" \
  "v_exp_f32 v137, v137\n\t" \
  "v_mfma_f32_32x32x8_bf16 v[158:173], v[230:231], v[220:221], v[158:173]\n\t" \
  "v_exp_f32 v138, v138\n\t" \
  "v_exp_f32 v139, v139\n\t" \
  "v_exp_f32 v140, v140\n\t" \
  "v_exp_f32 v141, v141\n\t" \
  "v_smfmac_f32_32x32x32_bf16 v[190:205], v[238:241], v[150:157], v255\n\t" \
  "v_exp_f32 v142, v142\n\t" \
  "v_exp_f32 v143, v143\n\t" \
  "v_exp_f32 v144, v144\n\t" \
  "v_exp_f32 v145, v145\n\t" \
  "v_smfmac_f32_32x32x32_bf16 v[174:189], v[242:245], v[134:141], v255\n\t" \
  "s_waitcnt lgkmcnt(0)\n\t" \
  "v_exp_f32 v146, v146\n\t" \
  "v_exp_f32 v147, v147\n\t" \
  "v_exp_f32 v148, v148\n\t" \
  "v_exp_f32 v149, v149\n\t" \
  "v_mfma_f32_32x32x16_bf16 v[126:141], v[222:225], v[206:209], 0\n\t" \
  "ds_read2_b64 v[226:229], v250 offset0:96 offset1:224\n\t" \
  "v_exp_f32 v158, v158\n\t" \
  "v_exp_f32 v159, v159\n\t" \
  "v_exp_f32 v160, v160\n\t" \
  "v_exp_f32 v161, v161\n\t" \
  "v_mfma_f32_32x32x8_bf16 v[126:141], v[232:233], v[214:215], v[126:141]\n\t" \
  "v_exp_f32 v162, v162\n\t" \
  "v_exp_f32 v163, v163\n\t" \
  "v_exp_f32 v164, v164\n\t" \
  "v_exp_f32 v165, v165\n\t" \
  "v_smfmac_f32_32x32x32_bf16 v[174:189], v[246:249], v[142:149], v255\n\t" \
  "ds_read_b128 v[238:241], v252 offset:2304\n\t" \
  "v_exp_f32 v166, v166\n\t" \
  "v_exp_f32 v167, v167\n\t" \
  "v_exp_f32 v168, v168\n\t" \
  "v_exp_f32 v169, v169\n\t" \
  "v_smfmac_f32_32x32x32_bf16 v[190:205], v[242:245], v[158:165], v255\n\t" \
  "ds_read_b128 v[242:245], v252 offset:3456\n\t" \
  "v_exp_f32 v170, v170\n\t" \
  "v_exp_f32 v171, v171\n\t" \
  "v_exp_f32 v172, v172\n\t" \
  "v_exp_f32 v173, v173\n\t" \
  "v_mfma_f32_32x32x16_bf16 v[150:165], v[222:225], v[210:213], 0\n\t" \
  "v_add_u32 v250, %[kinc], v250\n\t" \
  "v_mad_i32_i24 v251, v253, %[kinc], v251\n\t" \
  "v_exp_f32 v134, v134\n\t" \
  "v_exp_f32 v135, v135\n\t" \
  "v_exp_f32 v136, v136\n\t" \
  "v_exp_f32 v137, v137\n\t" \
  "v_mfma_f32_32x32x8_bf16 v[150:165], v[232:233], v[218:219], v[150:165]\n\t" \
  "v_exp_f32 v138, v138\n\t" \
  "v_exp_f32 v139, v139\n\t" \
  "v_exp_f32 v140, v140\n\t" \
  "v_exp_f32 v141, v141\n\t" \
  "v_smfmac_f32_32x32x32_bf16 v[190:205], v[246:249], v[166:173], v255\n\t" \
  "ds_read2st64_b32 v[230:231], v251 offset0:0 offset1:1\n\t" \
  "v_exp_f32 v126, v126\n\t" \
  "v_exp_f32 v127, v127\n\t" \
  "v_exp_f32 v128, v128\n\t" \
  "v_exp_f32 v129, v129\n\t" \
  "v_smfmac_f32_32x32x32_bf16 v[174:189], v[234:237], v[134:141], v255\n\t" \
  "s_waitcnt lgkmcnt(1)\n\t" \
  "v_exp_f32 v130, v130\n\t" \
  "v_exp_f32 v131, v131\n\t" \
  "v_exp_f32 v132, v132\n\t" \
  "v_exp_f32 v133, v133\n\t" \
  "v_mfma_f32_32x32x16_bf16 v[134:149], v[226:229], v[206:209], 0\n\t" \
  "ds_read2_b64 v[222:225], v250 offset0:0 offset1:128\n\t" \
  "v_exp_f32 v158, v158\n\t" \
  "v_exp_f32 v159, v159\n\t" \
  "v_exp_f32 v160, v160\n\t" \
  "v_exp_f32 v161, v161\n\t" \
  "v_mfma_f32_32x32x8_bf16 v[134:149], v[232:233], v[216:217], v[134:149]\n\t" \
  "v_exp_f32 v162, v162\n\t" \
  "v_exp_f32 v163, v163\n\t" \
  "v_exp_f32 v164, v164\n\t" \
  "v_exp_f32 v165, v165\n\t" \
  "v_smfmac_f32_32x32x32_bf16 v[174:189], v[238:241], v[126:133], v255\n\t" \
  "ds_read_b128 v[246:249], v252 offset:3488\n\t" \
  "v_exp_f32 v150, v150\n\t" \
  "v_exp_f32 v151, v151\n\t" \
  "v_exp_f32 v152, v152\n\t" \
  "v_exp_f32 v153, v153\n\t" \
  "v_mad_i32_i24 v252, v254, %[vinc], v252\n\t" \
  "v_smfmac_f32_32x32x32_bf16 v[190:205], v[234:237], v[158:165], v255\n\t" \
  "ds_read_b128 v[234:237], v252 offset:32\n\t" \
  "v_exp_f32 v154, v154\n\t" \
  "v_exp_f32 v155, v155\n\t" \
  "v_exp_f32 v156, v156\n\t" \
  "v_exp_f32 v157, v157\n\t" \
  "v_mfma_f32_32x32x16_bf16 v[158:173], v[226:229], v[210:213], 0\n\t" \
  "v_exp_f32 v134, v134\n\t" \
  "v_exp_f32 v135, v135\n\t" \
  "v_exp_f32 v136, v136\n\t" \
  "v_exp_f32 v137, v137\n\t" \
  "v_mfma_f32_32x32x8_bf16 v[158:173], v[232:233], v[220:221], v[158:173]\n\t" \
  "v_exp_f32 v138, v138\n\t" \
  "v_exp_f32 v139, v139\n\t" \
  "v_exp_f32 v140, v140\n\t" \
  "v_exp_f32 v141, v141\n\t" \
  "v_smfmac_f32_32x32x32_bf16 v[190:205], v[238:241], v[150:157], v255\n\t" \
  "v_exp_f32 v142, v142\n\t" \
  "v_exp_f32 v143, v143\n\t" \
  "v_exp_f32 v144, v144\n\t" \
  "v_exp_f32 v145, v145\n\t" \
  "v_smfmac_f32_32x32x32_bf16 v[174:189], v[242:245], v[134:141], v255\n\t" \
  "s_waitcnt lgkmcnt(0)\n\t" \
  "s_sub_u32 %[cnt], %[cnt], 1\n\t" \
  "v_exp_f32 v146, v146\n\t" \
  "v_exp_f32 v147, v147\n\t" \
  "v_exp_f32 v148, v148\n\t" \
  "v_exp_f32 v149, v149\n\t" \
  "v_mfma_f32_32x32x16_bf16 v[126:141], v[222:225], v[206:209], 0\n\t" \
  "ds_read2_b64 v[226:229], v250 offset0:32 offset1:160\n\t" \
  "v_exp_f32 v158, v158\n\t" \
  "v_exp_f32 v159, v159\n\t" \
  "v_exp_f32 v160, v160\n\t" \
  "v_exp_f32 v161, v161\n\t" \
  "v_mfma_f32_32x32x8_bf16 v[126:141], v[230:231], v[214:215], v[126:141]\n\t" \
  "v_exp_f32 v162, v162\n\t" \
  "v_exp_f32 v163, v163\n\t" \
  "v_exp_f32 v164, v164\n\t" \
  "v_exp_f32 v165, v165\n\t" \
  "v_smfmac_f32_32x32x32_bf16 v[174:189], v[246:249], v[142:149], v255\n\t" \
  "ds_read_b128 v[238:241], v252 offset:0\n\t" \
  "v_exp_f32 v166, v166\n\t" \
  "v_exp_f32 v167, v167\n\t" \
  "v_exp_f32 v168, v168\n\t" \
  "v_exp_f32 v169, v169\n\t" \
  "v_smfmac_f32_32x32x32_bf16 v[190:205], v[242:245], v[158:165], v255\n\t" \
  "ds_read_b128 v[242:245], v252 offset:1152\n\t" \
  "s_cmp_lg_u32 %[cnt], 0\n\t" \
  "v_exp_f32 v170, v170\n\t" \
  "v_exp_f32 v171, v171\n\t" \
  "v_exp_f32 v172, v172\n\t" \
  "v_exp_f32 v173, v173\n\t" \
  "s_cbranch_scc1 1b\n\t" \
  "s_nop 1\n\t" \
  "v_smfmac_f32_32x32x32_bf16 v[190:205], v[246:249], v[166:173], v255\n\t" \
  "s_waitcnt lgkmcnt(0)\n\t" \
  "s_nop 15\n\t" \
  ""
#define ATT_R_O "+{v[174:205]}"
#define ATT_R_Q "{v[206:221]}"
#define ATT_R_KA "+{v250}"
#define ATT_R_LA "+{v251}"
#define ATT_R_VA "+{v252}"
#define ATT_R_LINC "{v253}"
#define ATT_R_VINC "{v254}"
#define ATT_R_IDX "{v255}"
#define ATT_ZERO_O(x) asm volatile("v_mov_b64 v[174:175], 0\n\tv_mov_b64 v[176:177], 0\n\tv_mov_b64 v[178:179], 0\n\tv_mov_b64 v[180:181], 0\n\tv_mov_b64 v[182:183], 0\n\tv_mov_b64 v[184:185], 0\n\tv_mov_b64 v[186:187], 0\n\tv_mov_b64 v[188:189], 0\n\tv_mov_b64 v[190:191], 0\n\tv_mov_b64 v[192:193], 0\n\tv_mov_b64 v[194:195], 0\n\tv_mov_b64 v[196:197], 0\n\tv_mov_b64 v[198:199], 0\n\tv_mov_b64 v[200:201], 0\n\tv_mov_b64 v[202:203], 0\n\tv_mov_b64 v[204:205], 0\n\ts_nop 1" : "={v[174:205]}"(x))
#define ATT_FULL_ASM \
  "s_setprio 3\n\t" \
  "ds_read2_b64 v[222:225], v250 offset0:0 offset1:128\n\t" \
  "ds_read2st64_b32 v[230:231], v251 offset0:0 offset1:1\n\t" \
  "ds_read2_b64 v[226:229], v250 offset0:32 offset1:160\n\t" \
  "ds_read_b128 v[234:237], v252 offset:32\n\t" \
  "ds_read_b128 v[238:241], v252 offset:0\n\t" \
  "ds_read_b128 v[242:245], v252 offset:1152\n\t" \
  "v_mov_b64 v[174:175], 0\n\t" \
  "v_mov_b64 v[176:177], 0\n\t" \
  "v_mov_b64 v[178:179], 0\n\t" \
  "v_mov_b64 v[180:181], 0\n\t" \
  "v_mov_b64 v[182:183], 0\n\t" \
  "v_mov_b64 v[184:185], 0\n\t" \
  "v_mov_b64 v[186:187], 0\n\t" \
  "v_mov_b64 v[188:189], 0\n\t" \
  "v_mov_b64 v[190:191], 0\n\t" \
  "v_mov_b64 v[192:193], 0\n\t" \
  "v_mov_b64 v[194:195], 0\n\t" \
  "v_mov_b64 v[196:197], 0\n\t" \
  "v_mov_b64 v[198:199], 0\n\t" \
  "v_mov_b64 v[200:201], 0\n\t" \
  "v_mov_b64 v[202:203], 0\n\t" \
  "v_mov_b64 v[204:205], 0\n\t" \
  "s_waitcnt lgkmcnt(4)\n\t" \
  "v_mfma_f32_32x32x16_bf16 v[126:141], v[222:225], v[206:209], 0\n\t" \
  "v_mfma_f32_32x32x8_bf16 v[126:141], v[230:231], v[214:215], v[126:141]\n\t" \
  "s_waitcnt lgkmcnt(0)\n\t" \
  "s_nop 10\n\t" \
  "v_mfma_f32_32x32x16_bf16 v[150:165], v[222:225], v[210:213], 0\n\t" \
  "buffer_store_dwordx4 v[42:45], v10, %[srd], %[pd5] offen offset:0 sc1\n\t" \
  "v_exp_f32 v134, v134\n\t" \
  "v_exp_f32 v135, v135\n\t" \
  "v_exp_f32 v136, v136\n\t" \
  "v_exp_f32 v137, v137\n\t" \
  "v_mfma_f32_32x32x8_bf16 v[150:165], v[230:231], v[218:219], v[150:165]\n\t" \
  "v_exp_f32 v138, v138\n\t" \
  "v_exp_f32 v139, v139\n\t" \
  "v_exp_f32 v140, v140\n\t" \
  "v_exp_f32 v141, v141\n\t" \
  "ds_read2st64_b32 v[232:233], v251 offset0:2 offset1:3\n\t" \
  "v_exp_f32 v126, v126\n\t" \
  "v_exp_f32 v127, v127\n\t" \
  "v_exp_f32 v128, v128\n\t" \
  "v_exp_f32 v129, v129\n\t" \
  "v_smfmac_f32_32x32x32_bf16 v[174:189], v[234:237], v[134:141], v255\n\t" \
  "s_waitcnt lgkmcnt(1)\n\t" \
  "v_exp_f32 v130, v130\n\t" \
  "v_exp_f32 v131, v131\n\t" \
  "v_exp_f32 v132, v132\n\t" \
  "v_exp_f32 v133, v133\n\t" \
  "v_mfma_f32_32x32x16_bf16 v[134:149], v[226:229], v[206:209], 0\n\t" \
  "ds_read2_b64 v[222:225], v250 offset0:64 offset1:192\n\t" \
  "buffer_store_dwordx4 v[46:49], v10, %[srd], %[pd5] offen offset:1024 sc1\n\t" \
  "v_exp_f32 v158, v158\n\t" \
  "v_exp_f32 v159, v159\n\t" \
  "v_exp_f32 v160, v160\n\t" \
  "v_exp_f32 v161, v161\n\t" \
  "v_mfma_f32_32x32x8_bf16 v[134:149], v[230:231], v[216:217], v[134:149]\n\t" \
  "v_exp_f32 v162, v162\n\t" \
  "v_exp_f32 v163, v163\n\t" \
  "v_exp_f32 v164, v164\n\t" \
  "v_exp_f32 v165, v165\n\t" \
  "v_smfmac_f32_32x32x32_bf16 v[174:189], v[238:241], v[126:133], v255\n\t" \
  "ds_read_b128 v[246:249], v252 offset:1184\n\t" \
  "v_exp_f32 v150, v150\n\t" \
  "v_exp_f32 v151, v151\n\t" \
  "v_exp_f32 v152, v152\n\t" \
  "v_exp_f32 v153, v153\n\t" \
  "v_smfmac_f32_32x32x32_bf16 v[190:205], v[234:237], v[158:165], v255\n\t" \
  "ds_read_b128 v[234:237], v252 offset:2336\n\t" \
  "v_exp_f32 v154, v154\n\t" \
  "v_exp_f32 v155, v155\n\t" \
  "v_exp_f32 v156, v156\n\t" \
  "v_exp_f32 v157, v157\n\t" \
  "v_mfma_f32_32x32x16_bf16 v[158:173], v[226:229], v[210:213], 0\n\t" \
  "buffer_store_dwordx4 v[50:53], v10, %[srd], %[pd6] offen offset:0 sc1\n\t" \
  "v_exp_f32 v134, v134\n\t" \
  "v_exp_f32 v135, v135\n\t" \
  "v_exp_f32 v136, v136\n\t" \
  "v_exp_f32 v137, v137\n\t" \
  "v_mfma_f32_32x32x8_bf16 v[158:173], v[230:231], v[220:221], v[158:173]\n\t" \
  "v_exp_f32 v138, v138\n\t" \
  "v_exp_f32 v139, v139\n\t" \
  "v_exp_f32 v140, v140\n\t" \
  "v_exp_f32 v141, v141\n\t" \
  "v_smfmac_f32_32x32x32_bf16 v[190:205], v[238:241], v[150:157], v255\n\t" \
  "v_exp_f32 v142, v142\n\t" \
  "v_exp_f32 v143, v143\n\t" \
  "v_exp_f32 v144, v144\n\t" \
  "v_exp_f32 v145, v145\n\t" \
  "v_smfmac_f32_32x32x32_bf16 v[174:189], v[242:245], v[134:141], v255\n\t" \
  "s_waitcnt lgkmcnt(0)\n\t" \
  "v_exp_f32 v146, v146\n\t" \
  "v_exp_f32 v147, v147\n\t" \
  "v_exp_f32 v148, v148\n\t" \
  "v_exp_f32 v149, v149\n\t" \
  "v_mfma_f32_32x32x16_bf16 v[126:141], v[222:225], v[206:209], 0\n\t" \
  "ds_read2_b64 v[226:229], v250 offset0:96 offset1:224\n\t" \
  "buffer_store_dwordx4 v[54:57], v10, %[srd], %[pd6] offen offset:1024 sc1\n\t" \
  "v_exp_f32 v158, v158\n\t" \
  "v_exp_f32 v159, v159\n\t" \
  "v_exp_f32 v160, v160\n\t" \
  "v_exp_f32 v161, v161\n\t" \
  "v_mfma_f32_32x32x8_bf16 v[126:141], v[232:233], v[214:215], v[126:141]\n\t" \
  "v_exp_f32 v162, v162\n\t" \
  "v_exp_f32 v163, v163\n\t" \
  "v_exp_f32 v164, v164\n\t" \
  "v_exp_f32 v165, v165\n\t" \
  "v_smfmac_f32_32x32x32_bf16 v[174:189], v[246:249], v[142:149], v255\n\t" \
  "ds_read_b128 v[238:241], v252 offset:2304\n\t" \
  "v_exp_f32 v166, v166\n\t" \
  "v_exp_f32 v167, v167\n\t" \
  "v_exp_f32 v168, v168\n\t" \
  "v_exp_f32 v169, v169\n\t" \
  "v_smfmac_f32_32x32x32_bf16 v[190:205], v[242:245], v[158:165], v255\n\t" \
  "ds_read_b128 v[242:245], v252 offset:3456\n\t" \
  "v_exp_f32 v170, v170\n\t" \
  "v_exp_f32 v171, v171\n\t" \
  "v_exp_f32 v172, v172\n\t" \
  "v_exp_f32 v173, v173\n\t" \
  "v_mfma_f32_32x32x16_bf16 v[150:165], v[222:225], v[210:213], 0\n\t" \
  "v_add_u32 v250, %[kinc], v250\n\t" \
  "v_mad_i32_i24 v251, v253, %[kinc], v251\n\t" \
  "buffer_store_dwordx4 v[58:61], v10, %[srd], %[pd7] offen offset:0 sc1\n\t" \
  "v_exp_f32 v134, v134\n\t" \
  "v_exp_f32 v135, v135\n\t" \
  "v_exp_f32 v136, v136\n\t" \
  "v_exp_f32 v137, v137\n\t" \
  "v_mfma_f32_32x32x8_bf16 v[150:165], v[232:233], v[218:219], v[150:165]\n\t" \
  "v_exp_f32 v138, v138\n\t" \
  "v_exp_f32 v139, v139\n\t" \
  "v_exp_f32 v140, v140\n\t" \
  "v_exp_f32 v141, v141\n\t" \
  "v_smfmac_f32_32x32x32_bf16 v[190:205], v[246:249], v[166:173], v255\n\t" \
  "ds_read2st64_b32 v[230:231], v251 offset0:0 offset1:1\n\t" \
  "v_exp_f32 v126, v126\n\t" \
  "v_exp_f32 v127, v127\n\t" \
  "v_exp_f32 v128, v128\n\t" \
  "v_exp_f32 v129, v129\n\t" \
  "v_smfmac_f32_32x32x32_bf16 v[174:189], v[234:237], v[134:141], v255\n\t" \
  "s_waitcnt lgkmcnt(1)\n\t" \
  "v_exp_f32 v130, v130\n\t" \
  "v_exp_f32 v131, v131\n\t" \
  "v_exp_f32 v132, v132\n\t" \
  "v_exp_f32 v133, v133\n\t" \
  "v_mfma_f32_32x32x16_bf16 v[134:149], v[226:229], v[206:209], 0\n\t" \
  "ds_read2_b64 v[222:225], v250 offset0:0 offset1:128\n\t" \
  "buffer_store_dwordx4 v[62:65], v10, %[srd], %[pd7] offen offset:1024 sc1\n\t" \
  "v_exp_f32 v158, v158\n\t" \
  "v_exp_f32 v159, v159\n\t" \
  "v_exp_f32 v160, v160\n\t" \
  "v_exp_f32 v161, v161\n\t" \
  "v_mfma_f32_32x32x8_bf16 v[134:149], v[232:233], v[216:217], v[134:149]\n\t" \
  "v_exp_f32 v162, v162\n\t" \
  "v_exp_f32 v163, v163\n\t" \
  "v_exp_f32 v164, v164\n\t" \
  "v_exp_f32 v165, v165\n\t" \
  "v_smfmac_f32_32x32x32_bf16 v[174:189], v[238:241], v[126:133], v255\n\t" \
  "ds_read_b128 v[246:249], v252 offset:3488\n\t" \
  "v_exp_f32 v150, v150\n\t" \
  "v_exp_f32 v151, v151\n\t" \
  "v_exp_f32 v152, v152\n\t" \
  "v_exp_f32 v153, v153\n\t" \
  "v_mad_i32_i24 v252, v254, %[vinc], v252\n\t" \
  "v_smfmac_f32_32x32x32_bf16 v[190:205], v[234:237], v[158:165], v255\n\t" \
  "ds_read_b128 v[234:237], v252 offset:32\n\t" \
  "v_exp_f32 v154, v154\n\t" \
  "v_exp_f32 v155, v155\n\t" \
  "v_exp_f32 v156, v156\n\t" \
  "v_exp_f32 v157, v157\n\t" \
  "v_mfma_f32_32x32x16_bf16 v[158:173], v[226:229], v[210:213], 0\n\t" \
  "buffer_store_dwordx4 v[66:69], v10, %[srd], %[pd8] offen offset:0 sc1\n\t" \
  "v_exp_f32 v134, v134\n\t" \
  "v_exp_f32 v135, v135\n\t" \
  "v_exp_f32 v136, v136\n\t" \
  "v_exp_f32 v137, v137\n\t" \
  "v_mfma_f32_32x32x8_bf16 v[158:173], v[232:233], v[220:221], v[158:173]\n\t" \
  "v_exp_f32 v138, v138\n\t" \
  "v_exp_f32 v139, v139\n\t" \
  "v_exp_f32 v140, v140\n\t" \
  "v_exp_f32 v141, v141\n\t" \
  "v_smfmac_f32_32x32x32_bf16 v[190:205], v[238:241], v[150:157], v255\n\t" \
  "v_exp_f32 v142, v142\n\t" \
  "v_exp_f32 v143, v143\n\t" \
  "v_exp_f32 v144, v144\n\t" \
  "v_exp_f32 v145, v145\n\t" \
  "v_smfmac_f32_32x32x32_bf16 v[174:189], v[242:245], v[134:141], v255\n\t" \
  "s_waitcnt lgkmcnt(0)\n\t" \
  "v_exp_f32 v146, v146\n\t" \
  "v_exp_f32 v147, v147\n\t" \
  "v_exp_f32 v148, v148\n\t" \
  "v_exp_f32 v149, v149\n\t" \
  "v_mfma_f32_32x32x16_bf16 v[126:141], v[222:225], v[206:209], 0\n\t" \
  "ds_read2_b64 v[226:229], v250 offset0:32 offset1:160\n\t" \
  "buffer_store_dwordx4 v[70:73], v10, %[srd], %[pd8] offen offset:1024 sc1\n\t" \
  "v_exp_f32 v158, v158\n\t" \
  "v_exp_f32 v159, v159\n\t" \
  "v_exp_f32 v160, v160\n\t" \
  "v_exp_f32 v161, v161\n\t" \
  "v_mfma_f32_32x32x8_bf16 v[126:141], v[230:231], v[214:215], v[126:141]\n\t" \
  "v_exp_f32 v162, v162\n\t" \
  "v_exp_f32 v163, v163\n\t" \
  "v_exp_f32 v164, v164\n\t" \
  "v_exp_f32 v165, v165\n\t" \
  "v_smfmac_f32_32x32x32_bf16 v[174:189], v[246:249], v[142:149], v255\n\t" \
  "ds_read_b128 v[238:241], v252 offset:0\n\t" \
  "v_exp_f32 v166, v166\n\t" \
  "v_exp_f32 v167, v167\n\t" \
  "v_exp_f32 v168, v168\n\t" \
  "v_exp_f32 v169, v169\n\t" \
  "v_smfmac_f32_32x32x32_bf16 v[190:205], v[242:245], v[158:165], v255\n\t" \
  "ds_read_b128 v[242:245], v252 offset:1152\n\t" \
  "s_setprio 2\n\t" \
  "v_exp_f32 v170, v170\n\t" \
  "v_exp_f32 v171, v171\n\t" \
  "v_exp_f32 v172, v172\n\t" \
  "v_exp_f32 v173, v173\n\t" \
  "v_mfma_f32_32x32x16_bf16 v[150:165], v[222:225], v[210:213], 0\n\t" \
  "s_add_u32 m0, %[kdst], 0\n\t" \
  "s_add_u32 %[t0], %[ksrc], 0\n\t" \
  "buffer_load_dwordx4 v10, %[srd], %[t0] offen sc1 lds\n\t" \
  "v_exp_f32 v134, v134\n\t" \
  "v_exp_f32 v135, v135\n\t" \
  "v_exp_f32 v136, v136\n\t" \
  "v_exp_f32 v137, v137\n\t" \
  "v_mfma_f32_32x32x8_bf16 v[150:165], v[230:231], v[218:219], v[150:165]\n\t" \
  "v_exp_f32 v138, v138\n\t" \
  "v_exp_f32 v139, v139\n\t" \
  "v_exp_f32 v140, v140\n\t" \
  "v_exp_f32 v141, v141\n\t" \
  "v_smfmac_f32_32x32x32_bf16 v[190:205], v[246:249], v[166:173], v255\n\t" \
  "ds_read2st64_b32 v[232:233], v251 offset0:2 offset1:3\n\t" \
  "v_exp_f32 v126, v126\n\t" \
  "v_exp_f32 v127, v127\n\t" \
  "v_exp_f32 v128, v128\n\t" \
  "v_exp_f32 v129, v129\n\t" \
  "v_smfmac_f32_32x32x32_bf16 v[174:189], v[234:237], v[134:141], v255\n\t" \
  "s_waitcnt lgkmcnt(1)\n\t" \
  "s_add_u32 m0, %[kdst], 1024\n\t" \
  "s_add_u32 %[t0], %[ksrc], 16384\n\t" \
  "buffer_load_dwordx4 v10, %[srd], %[t0] offen sc1 lds\n\t" \
  "v_exp_f32 v130, v130\n\t" \
  "v_exp_f32 v131, v131\n\t" \
  "v_exp_f32 v132, v132\n\t" \
  "v_exp_f32 v133, v133\n\t" \
  "v_mfma_f32_32x32x16_bf16 v[134:149], v[226:229], v[206:209], 0\n\t" \
  "ds_read2_b64 v[222:225], v250 offset0:64 offset1:192\n\t" \
  "s_add_u32 m0, %[kdst], 2048\n\t" \
  "s_add_u32 %[t0], %[ksrc], 32768\n\t" \
  "buffer_load_dwordx4 v10, %[srd], %[t0] offen sc1 lds\n\t" \
  "v_exp_f32 v158, v158\n\t" \
  "v_exp_f32 v159, v159\n\t" \
  "v_exp_f32 v160, v160\n\t" \
  "v_exp_f32 v161, v161\n\t" \
  "v_mfma_f32_32x32x8_bf16 v[134:149], v[230:231], v[216:217], v[134:149]\n\t" \
  "v_exp_f32 v162, v162\n\t" \
  "v_exp_f32 v163, v163\n\t" \
  "v_exp_f32 v164, v164\n\t" \
  "v_exp_f32 v165, v165\n\t" \
  "v_smfmac_f32_32x32x32_bf16 v[174:189], v[238:241], v[126:133], v255\n\t" \
  "ds_read_b128 v[246:249], v252 offset:1184\n\t" \
  "v_exp_f32 v150, v150\n\t" \
  "v_exp_f32 v151, v151\n\t" \
  "v_exp_f32 v152, v152\n\t" \
  "v_exp_f32 v153, v153\n\t" \
  "v_smfmac_f32_32x32x32_bf16 v[190:205], v[234:237], v[158:165], v255\n\t" \
  "ds_read_b128 v[234:237], v252 offset:2336\n\t" \
  "s_add_u32 m0, %[kdst], 3072\n\t" \
  "s_add_u32 %[t0], %[ksrc], 49152\n\t" \
  "buffer_load_dwordx4 v10, %[srd], %[t0] offen sc1 lds\n\t" \
  "v_exp_f32 v154, v154\n\t" \
  "v_exp_f32 v155, v155\n\t" \
  "v_exp_f32 v156, v156\n\t" \
  "v_exp_f32 v157, v157\n\t" \
  "v_mfma_f32_32x32x16_bf16 v[158:173], v[226:229], v[210:213], 0\n\t" \
  "s_add_u32 m0, %[kdst], 4096\n\t" \
  "s_add_u32 %[t0], %[ksrc], 65536\n\t" \
  "buffer_load_dwordx4 v10, %[srd], %[t0] offen sc1 lds\n\t" \
  "v_exp_f32 v134, v134\n\t" \
  "v_exp_f32 v135, v135\n\t" \
  "v_exp_f32 v136, v136\n\t" \
  "v_exp_f32 v137, v137\n\t" \
  "v_mfma_f32_32x32x8_bf16 v[158:173], v[230:231], v[220:221], v[158:173]\n\t" \
  "v_exp_f32 v138, v138\n\t" \
  "v_exp_f32 v139, v139\n\t" \
  "v_exp_f32 v140, v140\n\t" \
  "v_exp_f32 v141, v141\n\t" \
  "v_smfmac_f32_32x32x32_bf16 v[190:205], v[238:241], v[150:157], v255\n\t" \
  "v_exp_f32 v142, v142\n\t" \
  "v_exp_f32 v143, v143\n\t" \
  "v_exp_f32 v144, v144\n\t" \
  "v_exp_f32 v145, v145\n\t" \
  "v_smfmac_f32_32x32x32_bf16 v[174:189], v[242:245], v[134:141], v255\n\t" \
  "s_waitcnt lgkmcnt(0)\n\t" \
  "s_add_u32 m0, %[vdst], 0\n\t" \
  "s_add_u32 %[t0], %[vsrc], 0\n\t" \
  "buffer_load_dwordx4 v10, %[srd], %[t0] offen sc1 lds\n\t" \
  "v_exp_f32 v146, v146\n\t" \
  "v_exp_f32 v147, v147\n\t" \
  "v_exp_f32 v148, v148\n\t" \
  "v_exp_f32 v149, v149\n\t" \
  "v_mfma_f32_32x32x16_bf16 v[126:141], v[222:225], v[206:209], 0\n\t" \
  "ds_read2_b64 v[226:229], v250 offset0:96 offset1:224\n\t" \
  "s_add_u32 m0, %[vdst], 1024\n\t" \
  "s_add_u32 %[t0], %[vsrc], 1024\n\t" \
  "buffer_load_dwordx4 v10, %[srd], %[t0] offen sc1 lds\n\t" \
  "v_exp_f32 v158, v158\n\t" \
  "v_exp_f32 v159, v159\n\t" \
  "v_exp_f32 v160, v160\n\t" \
  "v_exp_f32 v161, v161\n\t" \
  "v_mfma_f32_32x32x8_bf16 v[126:141], v[232:233], v[214:215], v[126:141]\n\t" \
  "v_exp_f32 v162, v162\n\t" \
  "v_exp_f32 v163, v163\n\t" \
  "v_exp_f32 v164, v164\n\t" \
  "v_exp_f32 v165, v165\n\t" \
  "v_smfmac_f32_32x32x32_bf16 v[174:189], v[246:249], v[142:149], v255\n\t" \
  "ds_read_b128 v[238:241], v252 offset:2304\n\t" \
  "v_exp_f32 v166, v166\n\t" \
  "v_exp_f32 v167, v167\n\t" \
  "v_exp_f32 v168, v168\n\t" \
  "v_exp_f32 v169, v169\n\t" \
  "v_smfmac_f32_32x32x32_bf16 v[190:205], v[242:245], v[158:165], v255\n\t" \
  "ds_read_b128 v[242:245], v252 offset:3456\n\t" \
  "s_add_u32 m0, %[vdst], 2048\n\t" \
  "s_add_u32 %[t0], %[vsrc], 2048\n\t" \
  "buffer_load_dwordx4 v10, %[srd], %[t0] offen sc1 lds\n\t" \
  "s_setprio 1\n\t" \
  "v_exp_f32 v170, v170\n\t" \
  "v_exp_f32 v171, v171\n\t" \
  "v_exp_f32 v172, v172\n\t" \
  "v_exp_f32 v173, v173\n\t" \
  "v_mfma_f32_32x32x16_bf16 v[150:165], v[222:225], v[210:213], 0\n\t" \
  "v_add_u32 v250, %[kinc], v250\n\t" \
  "v_mad_i32_i24 v251, v253, %[kinc], v251\n\t" \
  "s_add_u32 m0, %[vdst], 3072\n\t" \
  "s_add_u32 %[t0], %[vsrc], 3072\n\t" \
  "buffer_load_dwordx4 v10, %[srd], %[t0] offen sc1 lds\n\t" \
  "v_exp_f32 v134, v134\n\t" \
  "v_exp_f32 v135, v135\n\t" \
  "v_exp_f32 v136, v136\n\t" \
  "v_exp_f32 v137, v137\n\t" \
  "v_mfma_f32_32x32x8_bf16 v[150:165], v[232:233], v[218:219], v[150:165]\n\t" \
  "v_exp_f32 v138, v138\n\t" \
  "v_exp_f32 v139, v139\n\t" \
  "v_exp_f32 v140, v140\n\t" \
  "v_exp_f32 v141, v141\n\t" \
  "v_smfmac_f32_32x32x32_bf16 v[190:205], v[246:249], v[166:173], v255\n\t" \
  "ds_read2st64_b32 v[230:231], v251 offset0:0 offset1:1\n\t" \
  "v_exp_f32 v126, v126\n\t" \
  "v_exp_f32 v127, v127\n\t" \
  "v_exp_f32 v128, v128\n\t" \
  "v_exp_f32 v129, v129\n\t" \
  "v_smfmac_f32_32x32x32_bf16 v[174:189], v[234:237], v[134:141], v255\n\t" \
  "s_waitcnt lgkmcnt(1)\n\t" \
  "s_add_u32 m0, %[vdst], 4096\n\t" \
  "s_add_u32 %[t0], %[vsrc], 4096\n\t" \
  "buffer_load_dwordx4 v10, %[srd], %[t0] offen sc1 lds\n\t" \
  "v_exp_f32 v130, v130\n\t" \
  "v_exp_f32 v131, v131\n\t" \
  "v_exp_f32 v132, v132\n\t" \
  "v_exp_f32 v133, v133\n\t" \
  "v_mfma_f32_32x32x16_bf16 v[134:149], v[226:229], v[206:209], 0\n\t" \
  "ds_read2_b64 v[222:225], v250 offset0:0 offset1:128\n\t" \
  "v_exp_f32 v158, v158\n\t" \
  "v_exp_f32 v159, v159\n\t" \
  "v_exp_f32 v160, v160\n\t" \
  "v_exp_f32 v161, v161\n\t" \
  "v_mfma_f32_32x32x8_bf16 v[134:149], v[232:233], v[216:217], v[134:149]\n\t" \
  "v_exp_f32 v162, v162\n\t" \
  "v_exp_f32 v163, v163\n\t" \
  "v_exp_f32 v164, v164\n\t" \
  "v_exp_f32 v165, v165\n\t" \
  "v_smfmac_f32_32x32x32_bf16 v[174:189], v[238:241], v[126:133], v255\n\t" \
  "ds_read_b128 v[246:249], v252 offset:3488\n\t" \
  "v_exp_f32 v150, v150\n\t" \
  "v_exp_f32 v151, v151\n\t" \
  "v_exp_f32 v152, v152\n\t" \
  "v_exp_f32 v153, v153\n\t" \
  "v_mad_i32_i24 v252, v254, %[vinc], v252\n\t" \
  "v_smfmac_f32_32x32x32_bf16 v[190:205], v[234:237], v[158:165], v255\n\t" \
  "ds_read_b128 v[234:237], v252 offset:32\n\t" \
  "s_setprio 0\n\t" \
  "v_exp_f32 v154, v154\n\t" \
  "v_exp_f32 v155, v155\n\t" \
  "v_exp_f32 v156, v156\n\t" \
  "v_exp_f32 v157, v157\n\t" \
  "v_mfma_f32_32x32x16_bf16 v[158:173], v[226:229], v[210:213], 0\n\t" \
  "v_exp_f32 v134, v134\n\t" \
  "v_exp_f32 v135, v135\n\t" \
  "v_exp_f32 v136, v136\n\t" \
  "v_exp_f32 v137, v137\n\t" \
  "v_mfma_f32_32x32x8_bf16 v[158:173], v[232:233], v[220:221], v[158:173]\n\t" \
  "v_exp_f32 v138, v138\n\t" \
  "v_exp_f32 v139, v139\n\t" \
  "v_exp_f32 v140, v140\n\t" \
  "v_exp_f32 v141, v141\n\t" \
  "v_smfmac_f32_32x32x32_bf16 v[190:205], v[238:241], v[150:157], v255\n\t" \
  "v_exp_f32 v142, v142\n\t" \
  "v_exp_f32 v143, v143\n\t" \
  "v_exp_f32 v144, v144\n\t" \
  "v_exp_f32 v145, v145\n\t" \
  "v_smfmac_f32_32x32x32_bf16 v[174:189], v[242:245], v[134:141], v255\n\t" \
  "s_waitcnt lgkmcnt(0)\n\t" \
  "v_exp_f32 v146, v146\n\t" \
  "v_exp_f32 v147, v147\n\t" \
  "v_exp_f32 v148, v148\n\t" \
  "v_exp_f32 v149, v149\n\t" \
  "v_mfma_f32_32x32x16_bf16 v[126:141], v[222:225], v[206:209], 0\n\t" \
  "ds_read2_b64 v[226:229], v250 offset0:32 offset1:160\n\t" \
  "v_exp_f32 v158, v158\n\t" \
  "v_exp_f32 v159, v159\n\t" \
  "v_exp_f32 v160, v160\n\t" \
  "v_exp_f32 v161, v161\n\t" \
  "v_mfma_f32_32x32x8_bf16 v[126:141], v[230:231], v[214:215], v[126:141]\n\t" \
  "v_exp_f32 v162, v162\n\t" \
  "v_exp_f32 v163, v163\n\t" \
  "v_exp_f32 v164, v164\n\t" \
  "v_exp_f32 v165, v165\n\t" \
  "v_smfmac_f32_32x32x32_bf16 v[174:189], v[246:249], v[142:149], v255\n\t" \
  "ds_read_b128 v[238:241], v252 offset:0\n\t" \
  "v_exp_f32 v166, v166\n\t" \
  "v_exp_f32 v167, v167\n\t" \
  "v_exp_f32 v168, v168\n\t" \
  "v_exp_f32 v169, v169\n\t" \
  "v_smfmac_f32_32x32x32_bf16 v[190:205], v[242:245], v[158:165], v255\n\t" \
  "ds_read_b128 v[242:245], v252 offset:1152\n\t" \
  "v_exp_f32 v170, v170\n\t" \
  "v_exp_f32 v171, v171\n\t" \
  "v_exp_f32 v172, v172\n\t" \
  "v_exp_f32 v173, v173\n\t" \
  "s_waitcnt vmcnt(0)\n\t" \
  "s_barrier\n\t" \
  "s_mov_b64 exec, 1\n\t" \
  "v_mov_b32 v34, %[tag]\n\t" \
  "buffer_store_dword v34, off, %[srd], %[fdst] sc1\n\t" \
  "s_mov_b64 exec, -1\n\t" \
  "v_mfma_f32_32x32x16_bf16 v[150:165], v[222:225], v[210:213], 0\n\t" \
  "buffer_load_dwordx2 v[8:9], v11, %[srd], %[gsrc] offen sc1\n\t" \
  "v_exp_f32 v134, v134\n\t" \
  "v_exp_f32 v135, v135\n\t" \
  "v_exp_f32 v136, v136\n\t" \
  "v_exp_f32 v137, v137\n\t" \
  "v_mfma_f32_32x32x8_bf16 v[150:165], v[230:231], v[218:219], v[150:165]\n\t" \
  "v_exp_f32 v138, v138\n\t" \
  "v_exp_f32 v139, v139\n\t" \
  "v_exp_f32 v140, v140\n\t" \
  "v_exp_f32 v141, v141\n\t" \
  "v_smfmac_f32_32x32x32_bf16 v[190:205], v[246:249], v[166:173], v255\n\t" \
  "ds_read2st64_b32 v[232:233], v251 offset0:2 offset1:3\n\t" \
  "s_add_u32 %[t0], %[wsrc], 0\n\t" \
  "buffer_load_dwordx4 v[42:45], v10, %[srd], %[t0] offen sc1\n\t" \
  "v_exp_f32 v126, v126\n\t" \
  "v_exp_f32 v127, v127\n\t" \
  "v_exp_f32 v128, v128\n\t" \
  "v_exp_f32 v129, v129\n\t" \
  "v_smfmac_f32_32x32x32_bf16 v[174:189], v[234:237], v[134:141], v255\n\t" \
  "s_waitcnt lgkmcnt(1)\n\t" \
  "s_setprio 3\n\t" \
  "v_exp_f32 v130, v130\n\t" \
  "v_exp_f32 v131, v131\n\t" \
  "v_exp_f32 v132, v132\n\t" \
  "v_exp_f32 v133, v133\n\t" \
  "v_mfma_f32_32x32x16_bf16 v[134:149], v[226:229], v[206:209], 0\n\t" \
  "ds_read2_b64 v[222:225], v250 offset0:64 offset1:192\n\t" \
  "v_exp_f32 v158, v158\n\t" \
  "v_exp_f32 v159, v159\n\t" \
  "v_exp_f32 v160, v160\n\t" \
  "v_exp_f32 v161, v161\n\t" \
  "v_mfma_f32_32x32x8_bf16 v[134:149], v[230:231], v[216:217], v[134:149]\n\t" \
  "v_exp_f32 v162, v162\n\t" \
  "v_exp_f32 v163, v163\n\t" \
  "v_exp_f32 v164, v164\n\t" \
  "v_exp_f32 v165, v165\n\t" \
  "v_smfmac_f32_32x32x32_bf16 v[174:189], v[238:241], v[126:133], v255\n\t" \
  "ds_read_b128 v[246:249], v252 offset:1184\n\t" \
  "v_exp_f32 v150, v150\n\t" \
  "v_exp_f32 v151, v151\n\t" \
  "v_exp_f32 v152, v152\n\t" \
  "v_exp_f32 v153, v153\n\t" \
  "v_smfmac_f32_32x32x32_bf16 v[190:205], v[234:237], v[158:165], v255\n\t" \
  "ds_read_b128 v[234:237], v252 offset:2336\n\t" \
  "v_exp_f32 v154, v154\n\t" \
  "v_exp_f32 v155, v155\n\t" \
  "v_exp_f32 v156, v156\n\t" \
  "v_exp_f32 v157, v157\n\t" \
  "v_mfma_f32_32x32x16_bf16 v[158:173], v[226:229], v[210:213], 0\n\t" \
  "v_exp_f32 v134, v134\n\t" \
  "v_exp_f32 v135, v135\n\t" \
  "v_exp_f32 v136, v136\n\t" \
  "v_exp_f32 v137, v137\n\t" \
  "v_mfma_f32_32x32x8_bf16 v[158:173], v[230:231], v[220:221], v[158:173]\n\t" \
  "v_exp_f32 v138, v138\n\t" \
  "v_exp_f32 v139, v139\n\t" \
  "v_exp_f32 v140, v140\n\t" \
  "v_exp_f32 v141, v141\n\t" \
  "v_smfmac_f32_32x32x32_bf16 v[190:205], v[238:241], v[150:157], v255\n\t" \
  "s_add_u32 %[t0], %[wsrc], 1024\n\t" \
  "buffer_load_dwordx4 v[46:49], v10, %[srd], %[t0] offen sc1\n\t" \
  "v_exp_f32 v142, v142\n\t" \
  "v_exp_f32 v143, v143\n\t" \
  "v_exp_f32 v144, v144\n\t" \
  "v_exp_f32 v145, v145\n\t" \
  "v_smfmac_f32_32x32x32_bf16 v[174:189], v[242:245], v[134:141], v255\n\t" \
  "s_waitcnt lgkmcnt(0)\n\t" \
  "v_exp_f32 v146, v146\n\t" \
  "v_exp_f32 v147, v147\n\t" \
  "v_exp_f32 v148, v148\n\t" \
  "v_exp_f32 v149, v149\n\t" \
  "v_mfma_f32_32x32x16_bf16 v[126:141], v[222:225], v[206:209], 0\n\t" \
  "ds_read2_b64 v[226:229], v250 offset0:96 offset1:224\n\t" \
  "v_exp_f32 v158, v158\n\t" \
  "v_exp_f32 v159, v159\n\t" \
  "v_exp_f32 v160, v160\n\t" \
  "v_exp_f32 v161, v161\n\t" \
  "v_mfma_f32_32x32x8_bf16 v[126:141], v[232:233], v[214:215], v[126:141]\n\t" \
  "v_exp_f32 v162, v162\n\t" \
  "v_exp_f32 v163, v163\n\t" \
  "v_exp_f32 v164, v164\n\t" \
  "v_exp_f32 v165, v165\n\t" \
  "v_smfmac_f32_32x32x32_bf16 v[174:189], v[246:249], v[142:149], v255\n\t" \
  "ds_read_b128 v[238:241], v252 offset:2304\n\t" \
  "v_exp_f32 v166, v166\n\t" \
  "v_exp_f32 v167, v167\n\t" \
  "v_exp_f32 v168, v168\n\t" \
  "v_exp_f32 v169, v169\n\t" \
  "v_smfmac_f32_32x32x32_bf16 v[190:205], v[242:245], v[158:165], v255\n\t" \
  "ds_read_b128 v[242:245], v252 offset:3456\n\t" \
  "v_exp_f32 v170, v170\n\t" \
  "v_exp_f32 v171, v171\n\t" \
  "v_exp_f32 v172, v172\n\t" \
  "v_exp_f32 v173, v173\n\t" \
  "v_mfma_f32_32x32x16_bf16 v[150:165], v[222:225], v[210:213], 0\n\t" \
  "v_add_u32 v250, %[kinc], v250\n\t" \
  "v_mad_i32_i24 v251, v253, %[kinc], v251\n\t" \
  "v_exp_f32 v134, v134\n\t" \
  "v_exp_f32 v135, v135\n\t" \
  "v_exp_f32 v136, v136\n\t" \
  "v_exp_f32 v137, v137\n\t" \
  "v_mfma_f32_32x32x8_bf16 v[150:165], v[232:233], v[218:219], v[150:165]\n\t" \
  "v_exp_f32 v138, v138\n\t" \
  "v_exp_f32 v139, v139\n\t" \
  "v_exp_f32 v140, v140\n\t" \
  "v_exp_f32 v141, v141\n\t" \
  "v_smfmac_f32_32x32x32_bf16 v[190:205], v[246:249], v[166:173], v255\n\t" \
  "ds_read2st64_b32 v[230:231], v251 offset0:0 offset1:1\n\t" \
  "s_add_u32 %[t0], %[wsrc], 2048\n\t" \
  "buffer_load_dwordx4 v[50:53], v10, %[srd], %[t0] offen sc1\n\t" \
  "v_exp_f32 v126, v126\n\t" \
  "v_exp_f32 v127, v127\n\t" \
  "v_exp_f32 v128, v128\n\t" \
  "v_exp_f32 v129, v129\n\t" \
  "v_smfmac_f32_32x32x32_bf16 v[174:189], v[234:237], v[134:141], v255\n\t" \
  "s_waitcnt lgkmcnt(1)\n\t" \
  "v_exp_f32 v130, v130\n\t" \
  "v_exp_f32 v131, v131\n\t" \
  "v_exp_f32 v132, v132\n\t" \
  "v_exp_f32 v133, v133\n\t" \
  "v_mfma_f32_32x32x16_bf16 v[134:149], v[226:229], v[206:209], 0\n\t" \
  "ds_read2_b64 v[222:225], v250 offset0:0 offset1:128\n\t" \
  "v_exp_f32 v158, v158\n\t" \
  "v_exp_f32 v159, v159\n\t" \
  "v_exp_f32 v160, v160\n\t" \
  "v_exp_f32 v161, v161\n\t" \
  "v_mfma_f32_32x32x8_bf16 v[134:149], v[232:233], v[216:217], v[134:149]\n\t" \
  "v_exp_f32 v162, v162\n\t" \
  "v_exp_f32 v163, v163\n\t" \
  "v_exp_f32 v164, v164\n\t" \
  "v_exp_f32 v165, v165\n\t" \
  "v_smfmac_f32_32x32x32_bf16 v[174:189], v[238:241], v[126:133], v255\n\t" \
  "ds_read_b128 v[246:249], v252 offset:3488\n\t" \
  "v_exp_f32 v150, v150\n\t" \
  "v_exp_f32 v151, v151\n\t" \
  "v_exp_f32 v152, v152\n\t" \
  "v_exp_f32 v153, v153\n\t" \
  "v_mad_i32_i24 v252, v254, %[vinc], v252\n\t" \
  "v_smfmac_f32_32x32x32_bf16 v[190:205], v[234:237], v[158:165], v255\n\t" \
  "ds_read_b128 v[234:237], v252 offset:32\n\t" \
  "v_exp_f32 v154, v154\n\t" \
  "v_exp_f32 v155, v155\n\t" \
  "v_exp_f32 v156, v156\n\t" \
  "v_exp_f32 v157, v157\n\t" \
  "v_mfma_f32_32x32x16_bf16 v[158:173], v[226:229], v[210:213], 0\n\t" \
  "v_exp_f32 v134, v134\n\t" \
  "v_exp_f32 v135, v135\n\t" \
  "v_exp_f32 v136, v136\n\t" \
  "v_exp_f32 v137, v137\n\t" \
  "v_mfma_f32_32x32x8_bf16 v[158:173], v[232:233], v[220:221], v[158:173]\n\t" \
  "v_exp_f32 v138, v138\n\t" \
  "v_exp_f32 v139, v139\n\t" \
  "v_exp_f32 v140, v140\n\t" \
  "v_exp_f32 v141, v141\n\t" \
  "v_smfmac_f32_32x32x32_bf16 v[190:205], v[238:241], v[150:157], v255\n\t" \
  "s_add_u32 %[t0], %[wsrc], 3072\n\t" \
  "buffer_load_dwordx4 v[54:57], v10, %[srd], %[t0] offen sc1\n\t" \
  "v_exp_f32 v142, v142\n\t" \
  "v_exp_f32 v143, v143\n\t" \
  "v_exp_f32 v144, v144\n\t" \
  "v_exp_f32 v145, v145\n\t" \
  "v_smfmac_f32_32x32x32_bf16 v[174:189], v[242:245], v[134:141], v255\n\t" \
  "s_waitcnt lgkmcnt(0)\n\t" \
  "v_exp_f32 v146, v146\n\t" \
  "v_exp_f32 v147, v147\n\t" \
  "v_exp_f32 v148, v148\n\t" \
  "v_exp_f32 v149, v149\n\t" \
  "v_mfma_f32_32x32x16_bf16 v[126:141], v[222:225], v[206:209], 0\n\t" \
  "ds_read2_b64 v[226:229], v250 offset0:32 offset1:160\n\t" \
  "v_exp_f32 v158, v158\n\t" \
  "v_exp_f32 v159, v159\n\t" \
  "v_exp_f32 v160, v160\n\t" \
  "v_exp_f32 v161, v161\n\t" \
  "v_mfma_f32_32x32x8_bf16 v[126:141], v[230:231], v[214:215], v[126:141]\n\t" \
  "v_exp_f32 v162, v162\n\t" \
  "v_exp_f32 v163, v163\n\t" \
  "v_exp_f32 v164, v164\n\t" \
  "v_exp_f32 v165, v165\n\t" \
  "v_smfmac_f32_32x32x32_bf16 v[174:189], v[246:249], v[142:149], v255\n\t" \
  "ds_read_b128 v[238:241], v252 offset:0\n\t" \
  "v_exp_f32 v166, v166\n\t" \
  "v_exp_f32 v167, v167\n\t" \
  "v_exp_f32 v168, v168\n\t" \
  "v_exp_f32 v169, v169\n\t" \
  "v_smfmac_f32_32x32x32_bf16 v[190:205], v[242:245], v[158:165], v255\n\t" \
  "ds_read_b128 v[242:245], v252 offset:1152\n\t" \
  "v_exp_f32 v170, v170\n\t" \
  "v_exp_f32 v171, v171\n\t" \
  "v_exp_f32 v172, v172\n\t" \
  "v_exp_f32 v173, v173\n\t" \
  "v_mfma_f32_32x32x16_bf16 v[150:165], v[222:225], v[210:213], 0\n\t" \
  "v_exp_f32 v134, v134\n\t" \
  "v_exp_f32 v135, v135\n\t" \
  "v_exp_f32 v136, v136\n\t" \
  "v_exp_f32 v137, v137\n\t" \
  "v_mfma_f32_32x32x8_bf16 v[150:165], v[230:231], v[218:219], v[150:165]\n\t" \
  "v_exp_f32 v138, v138\n\t" \
  "v_exp_f32 v139, v139\n\t" \
  "v_exp_f32 v140, v140\n\t" \
  "v_exp_f32 v141, v141\n\t" \
  "v_smfmac_f32_32x32x32_bf16 v[190:205], v[246:249], v[166:173], v255\n\t" \
  "ds_read2st64_b32 v[232:233], v251 offset0:2 offset1:3\n\t" \
  "s_add_u32 %[t0], %[wsrc], 4096\n\t" \
  "buffer_load_dwordx4 v[58:61], v10, %[srd], %[t0] offen sc1\n\t" \
  "v_exp_f32 v126, v126\n\t" \
  "v_exp_f32 v127, v127\n\t" \
  "v_exp_f32 v128, v128\n\t" \
  "v_exp_f32 v129, v129\n\t" \
  "v_smfmac_f32_32x32x32_bf16 v[174:189], v[234:237], v[134:141], v255\n\t" \
  "s_waitcnt lgkmcnt(1)\n\t" \
  "v_exp_f32 v130, v130\n\t" \
  "v_exp_f32 v131, v131\n\t" \
  "v_exp_f32 v132, v132\n\t" \
  "v_exp_f32 v133, v133\n\t" \
  "v_mfma_f32_32x32x16_bf16 v[134:149], v[226:229], v[206:209], 0\n\t" \
  "ds_read2_b64 v[222:225], v250 offset0:64 offset1:192\n\t" \
  "v_exp_f32 v158, v158\n\t" \
  "v_exp_f32 v159, v159\n\t" \
  "v_exp_f32 v160, v160\n\t" \
  "v_exp_f32 v161, v161\n\t" \
  "v_mfma_f32_32x32x8_bf16 v[134:149], v[230:231], v[216:217], v[134:149]\n\t" \
  "v_exp_f32 v162, v162\n\t" \
  "v_exp_f32 v163, v163\n\t" \
  "v_exp_f32 v164, v164\n\t" \
  "v_exp_f32 v165, v165\n\t" \
  "v_smfmac_f32_32x32x32_bf16 v[174:189], v[238:241], v[126:133], v255\n\t" \
  "ds_read_b128 v[246:249], v252 offset:1184\n\t" \
  "v_exp_f32 v150, v150\n\t" \
  "v_exp_f32 v151, v151\n\t" \
  "v_exp_f32 v152, v152\n\t" \
  "v_exp_f32 v153, v153\n\t" \
  "v_smfmac_f32_32x32x32_bf16 v[190:205], v[234:237], v[158:165], v255\n\t" \
  "ds_read_b128 v[234:237], v252 offset:2336\n\t" \
  "v_exp_f32 v154, v154\n\t" \
  "v_exp_f32 v155, v155\n\t" \
  "v_exp_f32 v156, v156\n\t" \
  "v_exp_f32 v157, v157\n\t" \
  "v_mfma_f32_32x32x16_bf16 v[158:173], v[226:229], v[210:213], 0\n\t" \
  "v_exp_f32 v134, v134\n\t" \
  "v_exp_f32 v135, v135\n\t" \
  "v_exp_f32 v136, v136\n\t" \
  "v_exp_f32 v137, v137\n\t" \
  "v_mfma_f32_32x32x8_bf16 v[158:173], v[230:231], v[220:221], v[158:173]\n\t" \
  "v_exp_f32 v138, v138\n\t" \
  "v_exp_f32 v139, v139\n\t" \
  "v_exp_f32 v140, v140\n\t" \
  "v_exp_f32 v141, v141\n\t" \
  "v_smfmac_f32_32x32x32_bf16 v[190:205], v[238:241], v[150:157], v255\n\t" \
  "s_add_u32 %[t0], %[wsrc], 5120\n\t" \
  "buffer_load_dwordx4 v[62:65], v10, %[srd], %[t0] offen sc1\n\t" \
  "v_exp_f32 v142, v142\n\t" \
  "v_exp_f32 v143, v143\n\t" \
  "v_exp_f32 v144, v144\n\t" \
  "v_exp_f32 v145, v145\n\t" \
  "v_smfmac_f32_32x32x32_bf16 v[174:189], v[242:245], v[134:141], v255\n\t" \
  "s_waitcnt lgkmcnt(0)\n\t" \
  "v_exp_f32 v146, v146\n\t" \
  "v_exp_f32 v147, v147\n\t" \
  "v_exp_f32 v148, v148\n\t" \
  "v_exp_f32 v149, v149\n\t" \
  "v_mfma_f32_32x32x16_bf16 v[126:141], v[222:225], v[206:209], 0\n\t" \
  "ds_read2_b64 v[226:229], v250 offset0:96 offset1:224\n\t" \
  "v_exp_f32 v158, v158\n\t" \
  "v_exp_f32 v159, v159\n\t" \
  "v_exp_f32 v160, v160\n\t" \
  "v_exp_f32 v161, v161\n\t" \
  "v_mfma_f32_32x32x8_bf16 v[126:141], v[232:233], v[214:215], v[126:141]\n\t" \
  "v_exp_f32 v162, v162\n\t" \
  "v_exp_f32 v163, v163\n\t" \
  "v_exp_f32 v164, v164\n\t" \
  "v_exp_f32 v165, v165\n\t" \
  "v_smfmac_f32_32x32x32_bf16 v[174:189], v[246:249], v[142:149], v255\n\t" \
  "ds_read_b128 v[238:241], v252 offset:2304\n\t" \
  "v_exp_f32 v166, v166\n\t" \
  "v_exp_f32 v167, v167\n\t" \
  "v_exp_f32 v168, v168\n\t" \
  "v_exp_f32 v169, v169\n\t" \
  "v_smfmac_f32_32x32x32_bf16 v[190:205], v[242:245], v[158:165], v255\n\t" \
  "ds_read_b128 v[242:245], v252 offset:3456\n\t" \
  "v_exp_f32 v170, v170\n\t" \
  "v_exp_f32 v171, v171\n\t" \
  "v_exp_f32 v172, v172\n\t" \
  "v_exp_f32 v173, v173\n\t" \
  "v_mfma_f32_32x32x16_bf16 v[150:165], v[222:225], v[210:213], 0\n\t" \
  "v_add_u32 v250, %[kjmp], v250\n\t" \
  "v_mad_i32_i24 v251, v253, %[kjmp], v251\n\t" \
  "v_exp_f32 v134, v134\n\t" \
  "v_exp_f32 v135, v135\n\t" \
  "v_exp_f32 v136, v136\n\t" \
  "v_exp_f32 v137, v137\n\t" \
  "v_mfma_f32_32x32x8_bf16 v[150:165], v[232:233], v[218:219], v[150:165]\n\t" \
  "v_exp_f32 v138, v138\n\t" \
  "v_exp_f32 v139, v139\n\t" \
  "v_exp_f32 v140, v140\n\t" \
  "v_exp_f32 v141, v141\n\t" \
  "v_smfmac_f32_32x32x32_bf16 v[190:205], v[246:249], v[166:173], v255\n\t" \
  "ds_read2st64_b32 v[230:231], v251 offset0:0 offset1:1\n\t" \
  "s_add_u32 %[t0], %[wsrc], 6144\n\t" \
  "buffer_load_dwordx4 v[66:69], v10, %[srd], %[t0] offen sc1\n\t" \
  "v_exp_f32 v126, v126\n\t" \
  "v_exp_f32 v127, v127\n\t" \
  "v_exp_f32 v128, v128\n\t" \
  "v_exp_f32 v129, v129\n\t" \
  "v_smfmac_f32_32x32x32_bf16 v[174:189], v[234:237], v[134:141], v255\n\t" \
  "s_waitcnt lgkmcnt(1)\n\t" \
  "v_exp_f32 v130, v130\n\t" \
  "v_exp_f32 v131, v131\n\t" \
  "v_exp_f32 v132, v132\n\t" \
  "v_exp_f32 v133, v133\n\t" \
  "v_mfma_f32_32x32x16_bf16 v[134:149], v[226:229], v[206:209], 0\n\t" \
  "ds_read2_b64 v[222:225], v250 offset0:0 offset1:128\n\t" \
  "v_exp_f32 v158, v158\n\t" \
  "v_exp_f32 v159, v159\n\t" \
  "v_exp_f32 v160, v160\n\t" \
  "v_exp_f32 v161, v161\n\t" \
  "v_mfma_f32_32x32x8_bf16 v[134:149], v[232:233], v[216:217], v[134:149]\n\t" \
  "v_exp_f32 v162, v162\n\t" \
  "v_exp_f32 v163, v163\n\t" \
  "v_exp_f32 v164, v164\n\t" \
  "v_exp_f32 v165, v165\n\t" \
  "v_smfmac_f32_32x32x32_bf16 v[174:189], v[238:241], v[126:133], v255\n\t" \
  "ds_read_b128 v[246:249], v252 offset:3488\n\t" \
  "v_exp_f32 v150, v150\n\t" \
  "v_exp_f32 v151, v151\n\t" \
  "v_exp_f32 v152, v152\n\t" \
  "v_exp_f32 v153, v153\n\t" \
  "v_mad_i32_i24 v252, v254, %[vjmp], v252\n\t" \
  "v_smfmac_f32_32x32x32_bf16 v[190:205], v[234:237], v[158:165], v255\n\t" \
  "ds_read_b128 v[234:237], v252 offset:32\n\t" \
  "v_exp_f32 v154, v154\n\t" \
  "v_exp_f32 v155, v155\n\t" \
  "v_exp_f32 v156, v156\n\t" \
  "v_exp_f32 v157, v157\n\t" \
  "v_mfma_f32_32x32x16_bf16 v[158:173], v[226:229], v[210:213], 0\n\t" \
  "v_lshlrev_b32 v34, 4, v11\n\t" \
  "v_exp_f32 v134, v134\n\t" \
  "v_exp_f32 v135, v135\n\t" \
  "v_exp_f32 v136, v136\n\t" \
  "v_exp_f32 v137, v137\n\t" \
  "v_mfma_f32_32x32x8_bf16 v[158:173], v[232:233], v[220:221], v[158:173]\n\t" \
  "v_exp_f32 v138, v138\n\t" \
  "v_exp_f32 v139, v139\n\t" \
  "v_exp_f32 v140, v140\n\t" \
  "v_exp_f32 v141, v141\n\t" \
  "v_smfmac_f32_32x32x32_bf16 v[190:205], v[238:241], v[150:157], v255\n\t" \
  "s_add_u32 %[t0], %[wsrc], 7168\n\t" \
  "buffer_load_dwordx4 v[70:73], v10, %[srd], %[t0] offen sc1\n\t" \
  "v_exp_f32 v142, v142\n\t" \
  "v_exp_f32 v143, v143\n\t" \
  "v_exp_f32 v144, v144\n\t" \
  "v_exp_f32 v145, v145\n\t" \
  "v_smfmac_f32_32x32x32_bf16 v[174:189], v[242:245], v[134:141], v255\n\t" \
  "s_waitcnt lgkmcnt(0)\n\t" \
  "buffer_load_dword v35, v34, %[srd], %[fsrc] offen sc1\n\t" \
  "v_exp_f32 v146, v146\n\t" \
  "v_exp_f32 v147, v147\n\t" \
  "v_exp_f32 v148, v148\n\t" \
  "v_exp_f32 v149, v149\n\t" \
  "v_mfma_f32_32x32x16_bf16 v[126:141], v[222:225], v[206:209], 0\n\t" \
  "ds_read2_b64 v[226:229], v250 offset0:32 offset1:160\n\t" \
  "v_exp_f32 v158, v158\n\t" \
  "v_exp_f32 v159, v159\n\t" \
  "v_exp_f32 v160, v160\n\t" \
  "v_exp_f32 v161, v161\n\t" \
  "v_mfma_f32_32x32x8_bf16 v[126:141], v[230:231], v[214:215], v[126:141]\n\t" \
  "v_exp_f32 v162, v162\n\t" \
  "v_exp_f32 v163, v163\n\t" \
  "v_exp_f32 v164, v164\n\t" \
  "v_exp_f32 v165, v165\n\t" \
  "v_smfmac_f32_32x32x32_bf16 v[174:189], v[246:249], v[142:149], v255\n\t" \
  "ds_read_b128 v[238:241], v252 offset:0\n\t" \
  "v_exp_f32 v166, v166\n\t" \
  "v_exp_f32 v167, v167\n\t" \
  "v_exp_f32 v168, v168\n\t" \
  "v_exp_f32 v169, v169\n\t" \
  "v_smfmac_f32_32x32x32_bf16 v[190:205], v[242:245], v[158:165], v255\n\t" \
  "ds_read_b128 v[242:245], v252 offset:1152\n\t" \
  "v_exp_f32 v170, v170\n\t" \
  "v_exp_f32 v171, v171\n\t" \
  "v_exp_f32 v172, v172\n\t" \
  "v_exp_f32 v173, v173\n\t" \
  "s_mov_b32 %[t0], 0x10000\n\t" \
  "2:\n\t" \
  "s_waitcnt vmcnt(0)\n\t" \
  "v_cmp_ne_u32 vcc, %[tag], v35\n\t" \
  "s_cbranch_vccz 3f\n\t" \
  "s_sleep 8\n\t" \
  "buffer_load_dword v35, v34, %[srd], %[fsrc] offen sc1\n\t" \
  "s_sub_u32 %[t0], %[t0], 1\n\t" \
  "s_cbranch_scc0 2b\n\t" \
  "s_waitcnt vmcnt(0)\n\t" \
  "3:\n\t" \
  "v_mfma_f32_32x32x16_bf16 v[150:165], v[222:225], v[210:213], 0\n\t" \
  "s_add_u32 %[t0], %[psrc], 0\n\t" \
  "buffer_load_dwordx4 v[34:37], v10, %[srd], %[t0] offen sc1\n\t" \
  "v_exp_f32 v134, v134\n\t" \
  "v_exp_f32 v135, v135\n\t" \
  "v_exp_f32 v136, v136\n\t" \
  "v_exp_f32 v137, v137\n\t" \
  "v_mfma_f32_32x32x8_bf16 v[150:165], v[230:231], v[218:219], v[150:165]\n\t" \
  "v_exp_f32 v138, v138\n\t" \
  "v_exp_f32 v139, v139\n\t" \
  "v_exp_f32 v140, v140\n\t" \
  "v_exp_f32 v141, v141\n\t" \
  "v_smfmac_f32_32x32x32_bf16 v[190:205], v[246:249], v[166:173], v255\n\t" \
  "ds_read2st64_b32 v[232:233], v251 offset0:2 offset1:3\n\t" \
  "s_add_u32 %[t0], %[wsrc], 8192\n\t" \
  "buffer_load_dwordx4 v[74:77], v10, %[srd], %[t0] offen sc1\n\t" \
  "v_exp_f32 v126, v126\n\t" \
  "v_exp_f32 v127, v127\n\t" \
  "v_exp_f32 v128, v128\n\t" \
  "v_exp_f32 v129, v129\n\t" \
  "v_smfmac_f32_32x32x32_bf16 v[174:189], v[234:237], v[134:141], v255\n\t" \
  "s_waitcnt lgkmcnt(1)\n\t" \
  "v_exp_f32 v130, v130\n\t" \
  "v_exp_f32 v131, v131\n\t" \
  "v_exp_f32 v132, v132\n\t" \
  "v_exp_f32 v133, v133\n\t" \
  "v_mfma_f32_32x32x16_bf16 v[134:149], v[226:229], v[206:209], 0\n\t" \
  "ds_read2_b64 v[222:225], v250 offset0:64 offset1:192\n\t" \
  "v_exp_f32 v158, v158\n\t" \
  "v_exp_f32 v159, v159\n\t" \
  "v_exp_f32 v160, v160\n\t" \
  "v_exp_f32 v161, v161\n\t" \
  "v_mfma_f32_32x32x8_bf16 v[134:149], v[230:231], v[216:217], v[134:149]\n\t" \
  "v_exp_f32 v162, v162\n\t" \
  "v_exp_f32 v163, v163\n\t" \
  "v_exp_f32 v164, v164\n\t" \
  "v_exp_f32 v165, v165\n\t" \
  "v_smfmac_f32_32x32x32_bf16 v[174:189], v[238:241], v[126:133], v255\n\t" \
  "ds_read_b128 v[246:249], v252 offset:1184\n\t" \
  "s_add_u32 %[t0], %[psrc], 147456\n\t" \
  "buffer_load_dwordx4 v[114:117], v10, %[srd], %[t0] offen sc1\n\t" \
  "v_exp_f32 v150, v150\n\t" \
  "v_exp_f32 v151, v151\n\t" \
  "v_exp_f32 v152, v152\n\t" \
  "v_exp_f32 v153, v153\n\t" \
  "v_smfmac_f32_32x32x32_bf16 v[190:205], v[234:237], v[158:165], v255\n\t" \
  "ds_read_b128 v[234:237], v252 offset:2336\n\t" \
  "v_exp_f32 v154, v154\n\t" \
  "v_exp_f32 v155, v155\n\t" \
  "v_exp_f32 v156, v156\n\t" \
  "v_exp_f32 v157, v157\n\t" \
  "v_mfma_f32_32x32x16_bf16 v[158:173], v[226:229], v[210:213], 0\n\t" \
  "v_exp_f32 v134, v134\n\t" \
  "v_exp_f32 v135, v135\n\t" \
  "v_exp_f32 v136, v136\n\t" \
  "v_exp_f32 v137, v137\n\t" \
  "v_mfma_f32_32x32x8_bf16 v[158:173], v[230:231], v[220:221], v[158:173]\n\t" \
  "v_exp_f32 v138, v138\n\t" \
  "v_exp_f32 v139, v139\n\t" \
  "v_exp_f32 v140, v140\n\t" \
  "v_exp_f32 v141, v141\n\t" \
  "v_smfmac_f32_32x32x32_bf16 v[190:205], v[238:241], v[150:157], v255\n\t" \
  "s_add_u32 %[t0], %[wsrc], 9216\n\t" \
  "buffer_load_dwordx4 v[78:81], v10, %[srd], %[t0] offen sc1\n\t" \
  "v_exp_f32 v142, v142\n\t" \
  "v_exp_f32 v143, v143\n\t" \
  "v_exp_f32 v144, v144\n\t" \
  "v_exp_f32 v145, v145\n\t" \
  "v_smfmac_f32_32x32x32_bf16 v[174:189], v[242:245], v[134:141], v255\n\t" \
  "s_waitcnt lgkmcnt(0)\n\t" \
  "s_add_u32 %[t0], %[psrc], 294912\n\t" \
  "buffer_load_dwordx4 v[118:121], v10, %[srd], %[t0] offen sc1\n\t" \
  "v_exp_f32 v146, v146\n\t" \
  "v_exp_f32 v147, v147\n\t" \
  "v_exp_f32 v148, v148\n\t" \
  "v_exp_f32 v149, v149\n\t" \
  "v_mfma_f32_32x32x16_bf16 v[126:141], v[222:225], v[206:209], 0\n\t" \
  "ds_read2_b64 v[226:229], v250 offset0:96 offset1:224\n\t" \
  "v_exp_f32 v158, v158\n\t" \
  "v_exp_f32 v159, v159\n\t" \
  "v_exp_f32 v160, v160\n\t" \
  "v_exp_f32 v161, v161\n\t" \
  "v_mfma_f32_32x32x8_bf16 v[126:141], v[232:233], v[214:215], v[126:141]\n\t" \
  "v_exp_f32 v162, v162\n\t" \
  "v_exp_f32 v163, v163\n\t" \
  "v_exp_f32 v164, v164\n\t" \
  "v_exp_f32 v165, v165\n\t" \
  "v_smfmac_f32_32x32x32_bf16 v[174:189], v[246:249], v[142:149], v255\n\t" \
  "ds_read_b128 v[238:241], v252 offset:2304\n\t" \
  "v_exp_f32 v166, v166\n\t" \
  "v_exp_f32 v167, v167\n\t" \
  "v_exp_f32 v168, v168\n\t" \
  "v_exp_f32 v169, v169\n\t" \
  "v_smfmac_f32_32x32x32_bf16 v[190:205], v[242:245], v[158:165], v255\n\t" \
  "ds_read_b128 v[242:245], v252 offset:3456\n\t" \
  "v_exp_f32 v170, v170\n\t" \
  "v_exp_f32 v171, v171\n\t" \
  "v_exp_f32 v172, v172\n\t" \
  "v_exp_f32 v173, v173\n\t" \
  "v_mfma_f32_32x32x16_bf16 v[150:165], v[222:225], v[210:213], 0\n\t" \
  "v_add_u32 v250, %[kinc], v250\n\t" \
  "v_mad_i32_i24 v251, v253, %[kinc], v251\n\t" \
  "s_add_u32 %[t0], %[psrc], 442368\n\t" \
  "buffer_load_dwordx4 v[122:125], v10, %[srd], %[t0] offen sc1\n\t" \
  "v_exp_f32 v134, v134\n\t" \
  "v_exp_f32 v135, v135\n\t" \
  "v_exp_f32 v136, v136\n\t" \
  "v_exp_f32 v137, v137\n\t" \
  "v_mfma_f32_32x32x8_bf16 v[150:165], v[232:233], v[218:219], v[150:165]\n\t" \
  "v_exp_f32 v138, v138\n\t" \
  "v_exp_f32 v139, v139\n\t" \
  "v_exp_f32 v140, v140\n\t" \
  "v_exp_f32 v141, v141\n\t" \
  "v_smfmac_f32_32x32x32_bf16 v[190:205], v[246:249], v[166:173], v255\n\t" \
  "ds_read2st64_b32 v[230:231], v251 offset0:0 offset1:1\n\t" \
  "s_add_u32 %[t0], %[wsrc], 10240\n\t" \
  "buffer_load_dwordx4 v[82:85], v10, %[srd], %[t0] offen sc1\n\t" \
  "v_exp_f32 v126, v126\n\t" \
  "v_exp_f32 v127, v127\n\t" \
  "v_exp_f32 v128, v128\n\t" \
  "v_exp_f32 v129, v129\n\t" \
  "v_smfmac_f32_32x32x32_bf16 v[174:189], v[234:237], v[134:141], v255\n\t" \
  "s_waitcnt lgkmcnt(1)\n\t" \
  "v_exp_f32 v130, v130\n\t" \
  "v_exp_f32 v131, v131\n\t" \
  "v_exp_f32 v132, v132\n\t" \
  "v_exp_f32 v133, v133\n\t" \
  "v_mfma_f32_32x32x16_bf16 v[134:149], v[226:229], v[206:209], 0\n\t" \
  "ds_read2_b64 v[222:225], v250 offset0:0 offset1:128\n\t" \
  "v_exp_f32 v158, v158\n\t" \
  "v_exp_f32 v159, v159\n\t" \
  "v_exp_f32 v160, v160\n\t" \
  "v_exp_f32 v161, v161\n\t" \
  "v_mfma_f32_32x32x8_bf16 v[134:149], v[232:233], v[216:217], v[134:149]\n\t" \
  "v_exp_f32 v162, v162\n\t" \
  "v_exp_f32 v163, v163\n\t" \
  "v_exp_f32 v164, v164\n\t" \
  "v_exp_f32 v165, v165\n\t" \
  "v_smfmac_f32_32x32x32_bf16 v[174:189], v[238:241], v[126:133], v255\n\t" \
  "ds_read_b128 v[246:249], v252 offset:3488\n\t" \
  "v_exp_f32 v150, v150\n\t" \
  "v_exp_f32 v151, v151\n\t" \
  "v_exp_f32 v152, v152\n\t" \
  "v_exp_f32 v153, v153\n\t" \
  "v_mad_i32_i24 v252, v254, %[vinc], v252\n\t" \
  "v_smfmac_f32_32x32x32_bf16 v[190:205], v[234:237], v[158:165], v255\n\t" \
  "ds_read_b128 v[234:237], v252 offset:32\n\t" \
  "s_add_u32 %[t0], %[psrc], 589824\n\t" \
  "buffer_load_dwordx4 v[38:41], v10, %[srd], %[t0] offen sc1\n\t" \
  "v_exp_f32 v154, v154\n\t" \
  "v_exp_f32 v155, v155\n\t" \
  "v_exp_f32 v156, v156\n\t" \
  "v_exp_f32 v157, v157\n\t" \
  "v_mfma_f32_32x32x16_bf16 v[158:173], v[226:229], v[210:213], 0\n\t" \
  "v_exp_f32 v134, v134\n\t" \
  "v_exp_f32 v135, v135\n\t" \
  "v_exp_f32 v136, v136\n\t" \
  "v_exp_f32 v137, v137\n\t" \
  "v_mfma_f32_32x32x8_bf16 v[158:173], v[232:233], v[220:221], v[158:173]\n\t" \
  "v_exp_f32 v138, v138\n\t" \
  "v_exp_f32 v139, v139\n\t" \
  "v_exp_f32 v140, v140\n\t" \
  "v_exp_f32 v141, v141\n\t" \
  "v_smfmac_f32_32x32x32_bf16 v[190:205], v[238:241], v[150:157], v255\n\t" \
  "s_add_u32 %[t0], %[wsrc], 11264\n\t" \
  "buffer_load_dwordx4 v[86:89], v10, %[srd], %[t0] offen sc1\n\t" \
  "v_exp_f32 v142, v142\n\t" \
  "v_exp_f32 v143, v143\n\t" \
  "v_exp_f32 v144, v144\n\t" \
  "v_exp_f32 v145, v145\n\t" \
  "v_smfmac_f32_32x32x32_bf16 v[174:189], v[242:245], v[134:141], v255\n\t" \
  "s_waitcnt lgkmcnt(0)\n\t" \
  "v_exp_f32 v146, v146\n\t" \
  "v_exp_f32 v147, v147\n\t" \
  "s_waitcnt vmcnt(8)\n\t" \
  "v_mfma_f32_16x16x32_bf16 v[18:21], v[14:17], v[34:37], 0\n\t" \
  "v_exp_f32 v148, v148\n\t" \
  "v_exp_f32 v149, v149\n\t" \
  "v_mfma_f32_32x32x16_bf16 v[126:141], v[222:225], v[206:209], 0\n\t" \
  "ds_read2_b64 v[226:229], v250 offset0:32 offset1:160\n\t" \
  "s_add_u32 %[t0], %[psrc], 737280\n\t" \
  "buffer_load_dwordx4 v[110:113], v10, %[srd], %[t0] offen sc1\n\t" \
  "v_exp_f32 v158, v158\n\t" \
  "v_exp_f32 v159, v159\n\t" \
  "v_mfma_f32_16x16x32_bf16 v[22:25], v[12:15], v[34:37], 0\n\t" \
  "v_exp_f32 v160, v160\n\t" \
  "v_exp_f32 v161, v161\n\t" \
  "v_mfma_f32_32x32x8_bf16 v[126:141], v[230:231], v[214:215], v[126:141]\n\t" \
  "v_exp_f32 v162, v162\n\t" \
  "v_exp_f32 v163, v163\n\t" \
  "v_exp_f32 v164, v164\n\t" \
  "v_exp_f32 v165, v165\n\t" \
  "v_smfmac_f32_32x32x32_bf16 v[174:189], v[246:249], v[142:149], v255\n\t" \
  "ds_read_b128 v[238:241], v252 offset:0\n\t" \
  "v_exp_f32 v166, v166\n\t" \
  "v_exp_f32 v167, v167\n\t" \
  "v_exp_f32 v168, v168\n\t" \
  "v_exp_f32 v169, v169\n\t" \
  "v_smfmac_f32_32x32x32_bf16 v[190:205], v[242:245], v[158:165], v255\n\t" \
  "ds_read_b128 v[242:245], v252 offset:1152\n\t" \
  "s_setprio 2\n\t" \
  "v_exp_f32 v170, v170\n\t" \
  "v_exp_f32 v171, v171\n\t" \
  "v_exp_f32 v172, v172\n\t" \
  "v_exp_f32 v173, v173\n\t" \
  "v_mfma_f32_32x32x16_bf16 v[150:165], v[222:225], v[210:213], 0\n\t" \
  "v_exp_f32 v134, v134\n\t" \
  "v_exp_f32 v135, v135\n\t" \
  "s_waitcnt vmcnt(7)\n\t" \
  "v_mfma_f32_16x16x32_bf16 v[18:21], v[14:17], v[114:117], v[18:21]\n\t" \
  "v_exp_f32 v136, v136\n\t" \
  "v_exp_f32 v137, v137\n\t" \
  "v_mfma_f32_32x32x8_bf16 v[150:165], v[230:231], v[218:219], v[150:165]\n\t" \
  "v_exp_f32 v138, v138\n\t" \
  "v_exp_f32 v139, v139\n\t" \
  "v_exp_f32 v140, v140\n\t" \
  "v_exp_f32 v141, v141\n\t" \
  "v_smfmac_f32_32x32x32_bf16 v[190:205], v[246:249], v[166:173], v255\n\t" \
  "ds_read2st64_b32 v[232:233], v251 offset0:2 offset1:3\n\t" \
  "s_add_u32 %[t0], %[psrc], 884736\n\t" \
  "buffer_load_dwordx4 v[34:37], v10, %[srd], %[t0] offen sc1\n\t" \
  "s_add_u32 %[t0], %[wsrc], 12288\n\t" \
  "buffer_load_dwordx4 v[90:93], v10, %[srd], %[t0] offen sc1\n\t" \
  "v_exp_f32 v126, v126\n\t" \
  "v_exp_f32 v127, v127\n\t" \
  "v_mfma_f32_16x16x32_bf16 v[22:25], v[12:15], v[114:117], v[22:25]\n\t" \
  "v_exp_f32 v128, v128\n\t" \
  "v_exp_f32 v129, v129\n\t" \
  "v_smfmac_f32_32x32x32_bf16 v[174:189], v[234:237], v[134:141], v255\n\t" \
  "s_waitcnt lgkmcnt(1)\n\t" \
  "v_exp_f32 v130, v130\n\t" \
  "v_exp_f32 v131, v131\n\t" \
  "v_exp_f32 v132, v132\n\t" \
  "v_exp_f32 v133, v133\n\t" \
  "v_mfma_f32_32x32x16_bf16 v[134:149], v[226:229], v[206:209], 0\n\t" \
  "ds_read2_b64 v[222:225], v250 offset0:64 offset1:192\n\t" \
  "v_exp_f32 v158, v158\n\t" \
  "v_exp_f32 v159, v159\n\t" \
  "v_exp_f32 v160, v160\n\t" \
  "v_exp_f32 v161, v161\n\t" \
  "v_mfma_f32_32x32x8_bf16 v[134:149], v[230:231], v[216:217], v[134:149]\n\t" \
  "v_exp_f32 v162, v162\n\t" \
  "v_exp_f32 v163, v163\n\t" \
  "v_exp_f32 v164, v164\n\t" \
  "v_exp_f32 v165, v165\n\t" \
  "v_smfmac_f32_32x32x32_bf16 v[174:189], v[238:241], v[126:133], v255\n\t" \
  "ds_read_b128 v[246:249], v252 offset:1184\n\t" \
  "v_exp_f32 v150, v150\n\t" \
  "v_exp_f32 v151, v151\n\t" \
  "s_waitcnt vmcnt(7)\n\t" \
  "v_mfma_f32_16x16x32_bf16 v[18:21], v[14:17], v[118:121], v[18:21]\n\t" \
  "v_exp_f32 v152, v152\n\t" \
  "v_exp_f32 v153, v153\n\t" \
  "v_smfmac_f32_32x32x32_bf16 v[190:205], v[234:237], v[158:165], v255\n\t" \
  "ds_read_b128 v[234:237], v252 offset:2336\n\t" \
  "v_exp_f32 v154, v154\n\t" \
  "v_exp_f32 v155, v155\n\t" \
  "v_mfma_f32_16x16x32_bf16 v[22:25], v[12:15], v[118:121], v[22:25]\n\t" \
  "v_exp_f32 v156, v156\n\t" \
  "v_exp_f32 v157, v157\n\t" \
  "v_mfma_f32_32x32x16_bf16 v[158:173], v[226:229], v[210:213], 0\n\t" \
  "s_add_u32 %[t0], %[psrc], 1032192\n\t" \
  "buffer_load_dwordx4 v[114:117], v10, %[srd], %[t0] offen sc1\n\t" \
  "v_exp_f32 v134, v134\n\t" \
  "v_exp_f32 v135, v135\n\t" \
  "v_exp_f32 v136, v136\n\t" \
  "v_exp_f32 v137, v137\n\t" \
  "v_mfma_f32_32x32x8_bf16 v[158:173], v[230:231], v[220:221], v[158:173]\n\t" \
  "v_exp_f32 v138, v138\n\t" \
  "v_exp_f32 v139, v139\n\t" \
  "v_exp_f32 v140, v140\n\t" \
  "v_exp_f32 v141, v141\n\t" \
  "v_smfmac_f32_32x32x32_bf16 v[190:205], v[238:241], v[150:157], v255\n\t" \
  "s_add_u32 %[t0], %[wsrc], 13312\n\t" \
  "buffer_load_dwordx4 v[94:97], v10, %[srd], %[t0] offen sc1\n\t" \
  "v_exp_f32 v142, v142\n\t" \
  "v_exp_f32 v143, v143\n\t" \
  "v_exp_f32 v144, v144\n\t" \
  "v_exp_f32 v145, v145\n\t" \
  "v_smfmac_f32_32x32x32_bf16 v[174:189], v[242:245], v[134:141], v255\n\t" \
  "s_waitcnt lgkmcnt(0)\n\t" \
  "v_exp_f32 v146, v146\n\t" \
  "v_exp_f32 v147, v147\n\t" \
  "s_waitcnt vmcnt(8)\n\t" \
  "v_mfma_f32_16x16x32_bf16 v[18:21], v[14:17], v[122:125], v[18:21]\n\t" \
  "v_exp_f32 v148, v148\n\t" \
  "v_exp_f32 v149, v149\n\t" \
  "v_mfma_f32_32x32x16_bf16 v[126:141], v[222:225], v[206:209], 0\n\t" \
  "ds_read2_b64 v[226:229], v250 offset0:96 offset1:224\n\t" \
  "v_exp_f32 v158, v158\n\t" \
  "v_exp_f32 v159, v159\n\t" \
  "v_mfma_f32_16x16x32_bf16 v[22:25], v[12:15], v[122:125], v[22:25]\n\t" \
  "v_exp_f32 v160, v160\n\t" \
  "v_exp_f32 v161, v161\n\t" \
  "v_mfma_f32_32x32x8_bf16 v[126:141], v[232:233], v[214:215], v[126:141]\n\t" \
  "v_exp_f32 v162, v162\n\t" \
  "v_exp_f32 v163, v163\n\t" \
  "v_exp_f32 v164, v164\n\t" \
  "v_exp_f32 v165, v165\n\t" \
  "v_smfmac_f32_32x32x32_bf16 v[174:189], v[246:249], v[142:149], v255\n\t" \
  "ds_read_b128 v[238:241], v252 offset:2304\n\t" \
  "s_add_u32 %[t0], %[psrc], 1179648\n\t" \
  "buffer_load_dwordx4 v[118:121], v10, %[srd], %[t0] offen sc1\n\t" \
  "v_exp_f32 v166, v166\n\t" \
  "v_exp_f32 v167, v167\n\t" \
  "v_exp_f32 v168, v168\n\t" \
  "v_exp_f32 v169, v169\n\t" \
  "v_smfmac_f32_32x32x32_bf16 v[190:205], v[242:245], v[158:165], v255\n\t" \
  "ds_read_b128 v[242:245], v252 offset:3456\n\t" \
  "v_exp_f32 v170, v170\n\t" \
  "v_exp_f32 v171, v171\n\t" \
  "v_exp_f32 v172, v172\n\t" \
  "v_exp_f32 v173, v173\n\t" \
  "v_mfma_f32_32x32x16_bf16 v[150:165], v[222:225], v[210:213], 0\n\t" \
  "v_add_u32 v250, %[kinc], v250\n\t" \
  "v_mad_i32_i24 v251, v253, %[kinc], v251\n\t" \
  "v_exp_f32 v134, v134\n\t" \
  "v_exp_f32 v135, v135\n\t" \
  "v_exp_f32 v136, v136\n\t" \
  "v_exp_f32 v137, v137\n\t" \
  "v_mfma_f32_32x32x8_bf16 v[150:165], v[232:233], v[218:219], v[150:165]\n\t" \
  "v_exp_f32 v138, v138\n\t" \
  "v_exp_f32 v139, v139\n\t" \
  "v_exp_f32 v140, v140\n\t" \
  "v_exp_f32 v141, v141\n\t" \
  "v_smfmac_f32_32x32x32_bf16 v[190:205], v[246:249], v[166:173], v255\n\t" \
  "ds_read2st64_b32 v[230:231], v251 offset0:0 offset1:1\n\t" \
  "s_add_u32 %[t0], %[wsrc], 14336\n\t" \
  "buffer_load_dwordx4 v[98:101], v10, %[srd], %[t0] offen sc1\n\t" \
  "v_exp_f32 v126, v126\n\t" \
  "v_exp_f32 v127, v127\n\t" \
  "s_waitcnt vmcnt(8)\n\t" \
  "v_mfma_f32_16x16x32_bf16 v[18:21], v[14:17], v[38:41], v[18:21]\n\t" \
  "v_exp_f32 v128, v128\n\t" \
  "v_exp_f32 v129, v129\n\t" \
  "v_smfmac_f32_32x32x32_bf16 v[174:189], v[234:237], v[134:141], v255\n\t" \
  "s_waitcnt lgkmcnt(1)\n\t" \
  "s_add_u32 %[t0], %[psrc], 1327104\n\t" \
  "buffer_load_dwordx4 v[122:125], v10, %[srd], %[t0] offen sc1\n\t" \
  "v_exp_f32 v130, v130\n\t" \
  "v_exp_f32 v131, v131\n\t" \
  "v_mfma_f32_16x16x32_bf16 v[22:25], v[12:15], v[38:41], v[22:25]\n\t" \
  "v_exp_f32 v132, v132\n\t" \
  "v_exp_f32 v133, v133\n\t" \
  "v_mfma_f32_32x32x16_bf16 v[134:149], v[226:229], v[206:209], 0\n\t" \
  "ds_read2_b64 v[222:225], v250 offset0:0 offset1:128\n\t" \
  "v_exp_f32 v158, v158\n\t" \
  "v_exp_f32 v159, v159\n\t" \
  "v_exp_f32 v160, v160\n\t" \
  "v_exp_f32 v161, v161\n\t" \
  "v_mfma_f32_32x32x8_bf16 v[134:149], v[232:233], v[216:217], v[134:149]\n\t" \
  "v_exp_f32 v162, v162\n\t" \
  "v_exp_f32 v163, v163\n\t" \
  "v_exp_f32 v164, v164\n\t" \
  "v_exp_f32 v165, v165\n\t" \
  "v_smfmac_f32_32x32x32_bf16 v[174:189], v[238:241], v[126:133], v255\n\t" \
  "ds_read_b128 v[246:249], v252 offset:3488\n\t" \
  "v_exp_f32 v150, v150\n\t" \
  "v_exp_f32 v151, v151\n\t" \
  "v_exp_f32 v152, v152\n\t" \
  "v_exp_f32 v153, v153\n\t" \
  "v_mad_i32_i24 v252, v254, %[vinc], v252\n\t" \
  "v_smfmac_f32_32x32x32_bf16 v[190:205], v[234:237], v[158:165], v255\n\t" \
  "ds_read_b128 v[234:237], v252 offset:32\n\t" \
  "v_exp_f32 v154, v154\n\t" \
  "v_exp_f32 v155, v155\n\t" \
  "s_waitcnt vmcnt(7)\n\t" \
  "v_mfma_f32_16x16x32_bf16 v[18:21], v[14:17], v[110:113], v[18:21]\n\t" \
  "v_exp_f32 v156, v156\n\t" \
  "v_exp_f32 v157, v157\n\t" \
  "v_mfma_f32_32x32x16_bf16 v[158:173], v[226:229], v[210:213], 0\n\t" \
  "s_add_u32 %[t0], %[psrc], 1474560\n\t" \
  "buffer_load_dwordx4 v[38:41], v10, %[srd], %[t0] offen sc1\n\t" \
  "v_exp_f32 v134, v134\n\t" \
  "v_exp_f32 v135, v135\n\t" \
  "v_mfma_f32_16x16x32_bf16 v[22:25], v[12:15], v[110:113], v[22:25]\n\t" \
  "v_exp_f32 v136, v136\n\t" \
  "v_exp_f32 v137, v137\n\t" \
  "v_mfma_f32_32x32x8_bf16 v[158:173], v[232:233], v[220:221], v[158:173]\n\t" \
  "v_exp_f32 v138, v138\n\t" \
  "v_exp_f32 v139, v139\n\t" \
  "v_exp_f32 v140, v140\n\t" \
  "v_exp_f32 v141, v141\n\t" \
  "v_smfmac_f32_32x32x32_bf16 v[190:205], v[238:241], v[150:157], v255\n\t" \
  "s_add_u32 %[t0], %[wsrc], 15360\n\t" \
  "buffer_load_dwordx4 v[102:105], v10, %[srd], %[t0] offen sc1\n\t" \
  "v_exp_f32 v142, v142\n\t" \
  "v_exp_f32 v143, v143\n\t" \
  "v_exp_f32 v144, v144\n\t" \
  "v_exp_f32 v145, v145\n\t" \
  "v_smfmac_f32_32x32x32_bf16 v[174:189], v[242:245], v[134:141], v255\n\t" \
  "s_waitcnt lgkmcnt(0)\n\t" \
  "v_exp_f32 v146, v146\n\t" \
  "v_exp_f32 v147, v147\n\t" \
  "v_exp_f32 v148, v148\n\t" \
  "v_exp_f32 v149, v149\n\t" \
  "v_mfma_f32_32x32x16_bf16 v[126:141], v[222:225], v[206:209], 0\n\t" \
  "ds_read2_b64 v[226:229], v250 offset0:32 offset1:160\n\t" \
  "v_exp_f32 v158, v158\n\t" \
  "v_exp_f32 v159, v159\n\t" \
  "s_waitcnt vmcnt(8)\n\t" \
  "v_mfma_f32_16x16x32_bf16 v[18:21], v[14:17], v[34:37], v[18:21]\n\t" \
  "v_exp_f32 v160, v160\n\t" \
  "v_exp_f32 v161, v161\n\t" \
  "v_mfma_f32_32x32x8_bf16 v[126:141], v[230:231], v[214:215], v[126:141]\n\t" \
  "v_exp_f32 v162, v162\n\t" \
  "v_exp_f32 v163, v163\n\t" \
  "v_exp_f32 v164, v164\n\t" \
  "v_exp_f32 v165, v165\n\t" \
  "v_smfmac_f32_32x32x32_bf16 v[174:189], v[246:249], v[142:149], v255\n\t" \
  "ds_read_b128 v[238:241], v252 offset:0\n\t" \
  "v_exp_f32 v166, v166\n\t" \
  "v_exp_f32 v167, v167\n\t" \
  "v_mfma_f32_16x16x32_bf16 v[22:25], v[12:15], v[34:37], v[22:25]\n\t" \
  "v_exp_f32 v168, v168\n\t" \
  "v_exp_f32 v169, v169\n\t" \
  "v_smfmac_f32_32x32x32_bf16 v[190:205], v[242:245], v[158:165], v255\n\t" \
  "ds_read_b128 v[242:245], v252 offset:1152\n\t" \
  "s_add_u32 %[t0], %[psrc], 1622016\n\t" \
  "buffer_load_dwordx4 v[110:113], v10, %[srd], %[t0] offen sc1\n\t" \
  "v_exp_f32 v170, v170\n\t" \
  "v_exp_f32 v171, v171\n\t" \
  "v_exp_f32 v172, v172\n\t" \
  "v_exp_f32 v173, v173\n\t" \
  "v_mfma_f32_32x32x16_bf16 v[150:165], v[222:225], v[210:213], 0\n\t" \
  "v_exp_f32 v134, v134\n\t" \
  "v_exp_f32 v135, v135\n\t" \
  "v_exp_f32 v136, v136\n\t" \
  "v_exp_f32 v137, v137\n\t" \
  "v_mfma_f32_32x32x8_bf16 v[150:165], v[230:231], v[218:219], v[150:165]\n\t" \
  "v_exp_f32 v138, v138\n\t" \
  "v_exp_f32 v139, v139\n\t" \
  "v_exp_f32 v140, v140\n\t" \
  "v_exp_f32 v141, v141\n\t" \
  "v_smfmac_f32_32x32x32_bf16 v[190:205], v[246:249], v[166:173], v255\n\t" \
  "ds_read2st64_b32 v[232:233], v251 offset0:2 offset1:3\n\t" \
  "s_add_u32 %[t0], %[wsrc], 16384\n\t" \
  "buffer_load_dwordx4 v[106:109], v10, %[srd], %[t0] offen sc1\n\t" \
  "v_exp_f32 v126, v126\n\t" \
  "v_exp_f32 v127, v127\n\t" \
  "v_exp_f32 v128, v128\n\t" \
  "v_exp_f32 v129, v129\n\t" \
  "v_smfmac_f32_32x32x32_bf16 v[174:189], v[234:237], v[134:141], v255\n\t" \
  "s_waitcnt lgkmcnt(1)\n\t" \
  "v_exp_f32 v130, v130\n\t" \
  "v_exp_f32 v131, v131\n\t" \
  "s_waitcnt vmcnt(8)\n\t" \
  "v_mfma_f32_16x16x32_bf16 v[18:21], v[14:17], v[114:117], v[18:21]\n\t" \
  "v_exp_f32 v132, v132\n\t" \
  "v_exp_f32 v133, v133\n\t" \
  "v_mfma_f32_32x32x16_bf16 v[134:149], v[226:229], v[206:209], 0\n\t" \
  "ds_read2_b64 v[222:225], v250 offset0:64 offset1:192\n\t" \
  "s_add_u32 %[t0], %[psrc], 1769472\n\t" \
  "buffer_load_dwordx4 v[34:37], v10, %[srd], %[t0] offen sc1\n\t" \
  "v_exp_f32 v158, v158\n\t" \
  "v_exp_f32 v159, v159\n\t" \
  "v_mfma_f32_16x16x32_bf16 v[22:25], v[12:15], v[114:117], v[22:25]\n\t" \
  "v_exp_f32 v160, v160\n\t" \
  "v_exp_f32 v161, v161\n\t" \
  "v_mfma_f32_32x32x8_bf16 v[134:149], v[230:231], v[216:217], v[134:149]\n\t" \
  "v_exp_f32 v162, v162\n\t" \
  "v_exp_f32 v163, v163\n\t" \
  "v_exp_f32 v164, v164\n\t" \
  "v_exp_f32 v165, v165\n\t" \
  "v_smfmac_f32_32x32x32_bf16 v[174:189], v[238:241], v[126:133], v255\n\t" \
  "ds_read_b128 v[246:249], v252 offset:1184\n\t" \
  "v_exp_f32 v150, v150\n\t" \
  "v_exp_f32 v151, v151\n\t" \
  "v_exp_f32 v152, v152\n\t" \
  "v_exp_f32 v153, v153\n\t" \
  "v_smfmac_f32_32x32x32_bf16 v[190:205], v[234:237], v[158:165], v255\n\t" \
  "ds_read_b128 v[234:237], v252 offset:2336\n\t" \
  "v_exp_f32 v154, v154\n\t" \
  "v_exp_f32 v155, v155\n\t" \
  "v_exp_f32 v156, v156\n\t" \
  "v_exp_f32 v157, v157\n\t" \
  "v_mfma_f32_32x32x16_bf16 v[158:173], v[226:229], v[210:213], 0\n\t" \
  "v_exp_f32 v134, v134\n\t" \
  "v_exp_f32 v135, v135\n\t" \
  "s_waitcnt vmcnt(7)\n\t" \
  "v_mfma_f32_16x16x32_bf16 v[18:21], v[14:17], v[118:121], v[18:21]\n\t" \
  "v_exp_f32 v136, v136\n\t" \
  "v_exp_f32 v137, v137\n\t" \
  "v_mfma_f32_32x32x8_bf16 v[158:173], v[230:231], v[220:221], v[158:173]\n\t" \
  "v_exp_f32 v138, v138\n\t" \
  "v_exp_f32 v139, v139\n\t" \
  "v_exp_f32 v140, v140\n\t" \
  "v_exp_f32 v141, v141\n\t" \
  "v_smfmac_f32_32x32x32_bf16 v[190:205], v[238:241], v[150:157], v255\n\t" \
  "s_add_u32 %[t0], %[psrc], 1916928\n\t" \
  "buffer_load_dwordx4 v[114:117], v10, %[srd], %[t0] offen sc1\n\t" \
  "v_exp_f32 v142, v142\n\t" \
  "v_exp_f32 v143, v143\n\t" \
  "v_mfma_f32_16x16x32_bf16 v[22:25], v[12:15], v[118:121], v[22:25]\n\t" \
  "v_exp_f32 v144, v144\n\t" \
  "v_exp_f32 v145, v145\n\t" \
  "v_smfmac_f32_32x32x32_bf16 v[174:189], v[242:245], v[134:141], v255\n\t" \
  "s_waitcnt lgkmcnt(0)\n\t" \
  "v_exp_f32 v146, v146\n\t" \
  "v_exp_f32 v147, v147\n\t" \
  "v_exp_f32 v148, v148\n\t" \
  "v_exp_f32 v149, v149\n\t" \
  "v_mfma_f32_32x32x16_bf16 v[126:141], v[222:225], v[206:209], 0\n\t" \
  "ds_read2_b64 v[226:229], v250 offset0:96 offset1:224\n\t" \
  "v_exp_f32 v158, v158\n\t" \
  "v_exp_f32 v159, v159\n\t" \
  "v_exp_f32 v160, v160\n\t" \
  "v_exp_f32 v161, v161\n\t" \
  "v_mfma_f32_32x32x8_bf16 v[126:141], v[232:233], v[214:215], v[126:141]\n\t" \
  "v_exp_f32 v162, v162\n\t" \
  "v_exp_f32 v163, v163\n\t" \
  "v_exp_f32 v164, v164\n\t" \
  "v_exp_f32 v165, v165\n\t" \
  "v_smfmac_f32_32x32x32_bf16 v[174:189], v[246:249], v[142:149], v255\n\t" \
  "ds_read_b128 v[238:241], v252 offset:2304\n\t" \
  "v_exp_f32 v166, v166\n\t" \
  "v_exp_f32 v167, v167\n\t" \
  "s_waitcnt vmcnt(6)\n\t" \
  "v_mfma_f32_16x16x32_bf16 v[18:21], v[14:17], v[122:125], v[18:21]\n\t" \
  "v_exp_f32 v168, v168\n\t" \
  "v_exp_f32 v169, v169\n\t" \
  "v_smfmac_f32_32x32x32_bf16 v[190:205], v[242:245], v[158:165], v255\n\t" \
  "ds_read_b128 v[242:245], v252 offset:3456\n\t" \
  "v_exp_f32 v170, v170\n\t" \
  "v_exp_f32 v171, v171\n\t" \
  "v_mfma_f32_16x16x32_bf16 v[22:25], v[12:15], v[122:125], v[22:25]\n\t" \
  "v_exp_f32 v172, v172\n\t" \
  "v_exp_f32 v173, v173\n\t" \
  "v_mfma_f32_32x32x16_bf16 v[150:165], v[222:225], v[210:213], 0\n\t" \
  "v_add_u32 v250, %[kinc], v250\n\t" \
  "v_mad_i32_i24 v251, v253, %[kinc], v251\n\t" \
  "s_add_u32 %[t0], %[psrc], 2064384\n\t" \
  "buffer_load_dwordx4 v[118:121], v10, %[srd], %[t0] offen sc1\n\t" \
  "v_exp_f32 v134, v134\n\t" \
  "v_exp_f32 v135, v135\n\t" \
  "v_exp_f32 v136, v136\n\t" \
  "v_exp_f32 v137, v137\n\t" \
  "v_mfma_f32_32x32x8_bf16 v[150:165], v[232:233], v[218:219], v[150:165]\n\t" \
  "v_exp_f32 v138, v138\n\t" \
  "v_exp_f32 v139, v139\n\t" \
  "v_exp_f32 v140, v140\n\t" \
  "v_exp_f32 v141, v141\n\t" \
  "v_smfmac_f32_32x32x32_bf16 v[190:205], v[246:249], v[166:173], v255\n\t" \
  "ds_read2st64_b32 v[230:231], v251 offset0:0 offset1:1\n\t" \
  "v_exp_f32 v126, v126\n\t" \
  "v_exp_f32 v127, v127\n\t" \
  "v_exp_f32 v128, v128\n\t" \
  "v_exp_f32 v129, v129\n\t" \
  "v_smfmac_f32_32x32x32_bf16 v[174:189], v[234:237], v[134:141], v255\n\t" \
  "s_waitcnt lgkmcnt(1)\n\t" \
  "v_exp_f32 v130, v130\n\t" \
  "v_exp_f32 v131, v131\n\t" \
  "s_waitcnt vmcnt(6)\n\t" \
  "v_mfma_f32_16x16x32_bf16 v[18:21], v[14:17], v[38:41], v[18:21]\n\t" \
  "v_exp_f32 v132, v132\n\t" \
  "v_exp_f32 v133, v133\n\t" \
  "v_mfma_f32_32x32x16_bf16 v[134:149], v[226:229], v[206:209], 0\n\t" \
  "ds_read2_b64 v[222:225], v250 offset0:0 offset1:128\n\t" \
  "v_exp_f32 v158, v158\n\t" \
  "v_exp_f32 v159, v159\n\t" \
  "v_mfma_f32_16x16x32_bf16 v[22:25], v[12:15], v[38:41], v[22:25]\n\t" \
  "v_exp_f32 v160, v160\n\t" \
  "v_exp_f32 v161, v161\n\t" \
  "v_mfma_f32_32x32x8_bf16 v[134:149], v[232:233], v[216:217], v[134:149]\n\t" \
  "v_exp_f32 v162, v162\n\t" \
  "v_exp_f32 v163, v163\n\t" \
  "v_exp_f32 v164, v164\n\t" \
  "v_exp_f32 v165, v165\n\t" \
  "v_smfmac_f32_32x32x32_bf16 v[174:189], v[238:241], v[126:133], v255\n\t" \
  "ds_read_b128 v[246:249], v252 offset:3488\n\t" \
  "s_add_u32 %[t0], %[psrc], 2211840\n\t" \
  "buffer_load_dwordx4 v[122:125], v10, %[srd], %[t0] offen sc1\n\t" \
  "v_exp_f32 v150, v150\n\t" \
  "v_exp_f32 v151, v151\n\t" \
  "v_exp_f32 v152, v152\n\t" \
  "v_exp_f32 v153, v153\n\t" \
  "v_mad_i32_i24 v252, v254, %[vinc], v252\n\t" \
  "v_smfmac_f32_32x32x32_bf16 v[190:205], v[234:237], v[158:165], v255\n\t" \
  "ds_read_b128 v[234:237], v252 offset:32\n\t" \
  "v_exp_f32 v154, v154\n\t" \
  "v_exp_f32 v155, v155\n\t" \
  "v_exp_f32 v156, v156\n\t" \
  "v_exp_f32 v157, v157\n\t" \
  "v_mfma_f32_32x32x16_bf16 v[158:173], v[226:229], v[210:213], 0\n\t" \
  "v_exp_f32 v134, v134\n\t" \
  "v_exp_f32 v135, v135\n\t" \
  "v_exp_f32 v136, v136\n\t" \
  "v_exp_f32 v137, v137\n\t" \
  "v_mfma_f32_32x32x8_bf16 v[158:173], v[232:233], v[220:221], v[158:173]\n\t" \
  "v_exp_f32 v138, v138\n\t" \
  "v_exp_f32 v139, v139\n\t" \
  "v_exp_f32 v140, v140\n\t" \
  "v_exp_f32 v141, v141\n\t" \
  "v_smfmac_f32_32x32x32_bf16 v[190:205], v[238:241], v[150:157], v255\n\t" \
  "v_exp_f32 v142, v142\n\t" \
  "v_exp_f32 v143, v143\n\t" \
  "s_waitcnt vmcnt(5)\n\t" \
  "v_mfma_f32_16x16x32_bf16 v[18:21], v[14:17], v[110:113], v[18:21]\n\t" \
  "v_exp_f32 v144, v144\n\t" \
  "v_exp_f32 v145, v145\n\t" \
  "v_smfmac_f32_32x32x32_bf16 v[174:189], v[242:245], v[134:141], v255\n\t" \
  "s_waitcnt lgkmcnt(0)\n\t" \
  "s_add_u32 %[t0], %[p9src], 0\n\t" \
  "buffer_load_dwordx4 v[38:41], v10, %[srd], %[t0] offen sc1\n\t" \
  "v_exp_f32 v146, v146\n\t" \
  "v_exp_f32 v147, v147\n\t" \
  "v_mfma_f32_16x16x32_bf16 v[22:25], v[12:15], v[110:113], v[22:25]\n\t" \
  "s_add_u32 %[t0], %[wsrc], 17408\n\t" \
  "buffer_load_dwordx4 v[110:113], v10, %[srd], %[t0] offen sc1\n\t" \
  "v_exp_f32 v148, v148\n\t" \
  "v_exp_f32 v149, v149\n\t" \
  "v_mfma_f32_32x32x16_bf16 v[126:141], v[222:225], v[206:209], 0\n\t" \
  "ds_read2_b64 v[226:229], v250 offset0:32 offset1:160\n\t" \
  "v_exp_f32 v158, v158\n\t" \
  "v_exp_f32 v159, v159\n\t" \
  "v_exp_f32 v160, v160\n\t" \
  "v_exp_f32 v161, v161\n\t" \
  "v_mfma_f32_32x32x8_bf16 v[126:141], v[230:231], v[214:215], v[126:141]\n\t" \
  "v_exp_f32 v162, v162\n\t" \
  "v_exp_f32 v163, v163\n\t" \
  "v_exp_f32 v164, v164\n\t" \
  "v_exp_f32 v165, v165\n\t" \
  "v_smfmac_f32_32x32x32_bf16 v[174:189], v[246:249], v[142:149], v255\n\t" \
  "ds_read_b128 v[238:241], v252 offset:0\n\t" \
  "v_exp_f32 v166, v166\n\t" \
  "v_exp_f32 v167, v167\n\t" \
  "v_exp_f32 v168, v168\n\t" \
  "v_exp_f32 v169, v169\n\t" \
  "v_smfmac_f32_32x32x32_bf16 v[190:205], v[242:245], v[158:165], v255\n\t" \
  "ds_read_b128 v[242:245], v252 offset:1152\n\t" \
  "s_setprio 1\n\t" \
  "v_exp_f32 v170, v170\n\t" \
  "v_exp_f32 v171, v171\n\t" \
  "s_waitcnt vmcnt(5)\n\t" \
  "v_mfma_f32_16x16x32_bf16 v[18:21], v[14:17], v[34:37], v[18:21]\n\t" \
  "v_exp_f32 v172, v172\n\t" \
  "v_exp_f32 v173, v173\n\t" \
  "v_mfma_f32_32x32x16_bf16 v[150:165], v[222:225], v[210:213], 0\n\t" \
  "v_exp_f32 v134, v134\n\t" \
  "v_exp_f32 v135, v135\n\t" \
  "v_mfma_f32_16x16x32_bf16 v[22:25], v[12:15], v[34:37], v[22:25]\n\t" \
  "v_exp_f32 v136, v136\n\t" \
  "v_exp_f32 v137, v137\n\t" \
  "v_mfma_f32_32x32x8_bf16 v[150:165], v[230:231], v[218:219], v[150:165]\n\t" \
  "v_exp_f32 v138, v138\n\t" \
  "v_exp_f32 v139, v139\n\t" \
  "v_exp_f32 v140, v140\n\t" \
  "v_exp_f32 v141, v141\n\t" \
  "v_smfmac_f32_32x32x32_bf16 v[190:205], v[246:249], v[166:173], v255\n\t" \
  "ds_read2st64_b32 v[232:233], v251 offset0:2 offset1:3\n\t" \
  "s_add_u32 %[t0], %[p9src], 147456\n\t" \
  "buffer_load_dwordx4 v[34:37], v10, %[srd], %[t0] offen sc1\n\t" \
  "v_exp_f32 v126, v126\n\t" \
  "v_exp_f32 v127, v127\n\t" \
  "v_exp_f32 v128, v128\n\t" \
  "v_exp_f32 v129, v129\n\t" \
  "v_smfmac_f32_32x32x32_bf16 v[174:189], v[234:237], v[134:141], v255\n\t" \
  "s_waitcnt lgkmcnt(1)\n\t" \
  "v_exp_f32 v130, v130\n\t" \
  "v_exp_f32 v131, v131\n\t" \
  "v_exp_f32 v132, v132\n\t" \
  "v_exp_f32 v133, v133\n\t" \
  "v_mfma_f32_32x32x16_bf16 v[134:149], v[226:229], v[206:209], 0\n\t" \
  "ds_read2_b64 v[222:225], v250 offset0:64 offset1:192\n\t" \
  "v_exp_f32 v158, v158\n\t" \
  "v_exp_f32 v159, v159\n\t" \
  "s_waitcnt vmcnt(5)\n\t" \
  "v_mfma_f32_16x16x32_bf16 v[18:21], v[14:17], v[114:117], v[18:21]\n\t" \
  "v_exp_f32 v160, v160\n\t" \
  "v_exp_f32 v161, v161\n\t" \
  "v_mfma_f32_32x32x8_bf16 v[134:149], v[230:231], v[216:217], v[134:149]\n\t" \
  "v_exp_f32 v162, v162\n\t" \
  "v_exp_f32 v163, v163\n\t" \
  "v_exp_f32 v164, v164\n\t" \
  "v_exp_f32 v165, v165\n\t" \
  "v_smfmac_f32_32x32x32_bf16 v[174:189], v[238:241], v[126:133], v255\n\t" \
  "ds_read_b128 v[246:249], v252 offset:1184\n\t" \
  "v_exp_f32 v150, v150\n\t" \
  "v_exp_f32 v151, v151\n\t" \
  "v_mfma_f32_16x16x32_bf16 v[22:25], v[12:15], v[114:117], v[22:25]\n\t" \
  "s_add_u32 %[t0], %[w9src], 0\n\t" \
  "buffer_load_dwordx4 v[114:117], v10, %[srd], %[t0] offen sc1\n\t" \
  "v_exp_f32 v152, v152\n\t" \
  "v_exp_f32 v153, v153\n\t" \
  "v_smfmac_f32_32x32x32_bf16 v[190:205], v[234:237], v[158:165], v255\n\t" \
  "ds_read_b128 v[234:237], v252 offset:2336\n\t" \
  "v_exp_f32 v154, v154\n\t" \
  "v_exp_f32 v155, v155\n\t" \
  "v_exp_f32 v156, v156\n\t" \
  "v_exp_f32 v157, v157\n\t" \
  "v_mfma_f32_32x32x16_bf16 v[158:173], v[226:229], v[210:213], 0\n\t" \
  "v_exp_f32 v134, v134\n\t" \
  "v_exp_f32 v135, v135\n\t" \
  "v_exp_f32 v136, v136\n\t" \
  "v_exp_f32 v137, v137\n\t" \
  "v_mfma_f32_32x32x8_bf16 v[158:173], v[230:231], v[220:221], v[158:173]\n\t" \
  "v_exp_f32 v138, v138\n\t" \
  "v_exp_f32 v139, v139\n\t" \
  "v_exp_f32 v140, v140\n\t" \
  "v_exp_f32 v141, v141\n\t" \
  "v_smfmac_f32_32x32x32_bf16 v[190:205], v[238:241], v[150:157], v255\n\t" \
  "v_exp_f32 v142, v142\n\t" \
  "v_exp_f32 v143, v143\n\t" \
  "v_exp_f32 v144, v144\n\t" \
  "v_exp_f32 v145, v145\n\t" \
  "v_smfmac_f32_32x32x32_bf16 v[174:189], v[242:245], v[134:141], v255\n\t" \
  "s_waitcnt lgkmcnt(0)\n\t" \
  "v_exp_f32 v146, v146\n\t" \
  "v_exp_f32 v147, v147\n\t" \
  "s_waitcnt vmcnt(5)\n\t" \
  "v_mfma_f32_16x16x32_bf16 v[18:21], v[14:17], v[118:121], v[18:21]\n\t" \
  "v_exp_f32 v148, v148\n\t" \
  "v_exp_f32 v149, v149\n\t" \
  "v_mfma_f32_32x32x16_bf16 v[126:141], v[222:225], v[206:209], 0\n\t" \
  "ds_read2_b64 v[226:229], v250 offset0:96 offset1:224\n\t" \
  "v_exp_f32 v158, v158\n\t" \
  "v_exp_f32 v159, v159\n\t" \
  "v_mfma_f32_16x16x32_bf16 v[22:25], v[12:15], v[118:121], v[22:25]\n\t" \
  "s_add_u32 %[t0], %[w9src], 8192\n\t" \
  "buffer_load_dwordx4 v[118:121], v10, %[srd], %[t0] offen sc1\n\t" \
  "v_exp_f32 v160, v160\n\t" \
  "v_exp_f32 v161, v161\n\t" \
  "v_mfma_f32_32x32x8_bf16 v[126:141], v[232:233], v[214:215], v[126:141]\n\t" \
  "v_exp_f32 v162, v162\n\t" \
  "v_exp_f32 v163, v163\n\t" \
  "v_exp_f32 v164, v164\n\t" \
  "v_exp_f32 v165, v165\n\t" \
  "v_smfmac_f32_32x32x32_bf16 v[174:189], v[246:249], v[142:149], v255\n\t" \
  "ds_read_b128 v[238:241], v252 offset:2304\n\t" \
  "v_exp_f32 v166, v166\n\t" \
  "v_exp_f32 v167, v167\n\t" \
  "v_exp_f32 v168, v168\n\t" \
  "v_exp_f32 v169, v169\n\t" \
  "v_smfmac_f32_32x32x32_bf16 v[190:205], v[242:245], v[158:165], v255\n\t" \
  "ds_read_b128 v[242:245], v252 offset:3456\n\t" \
  "v_exp_f32 v170, v170\n\t" \
  "v_exp_f32 v171, v171\n\t" \
  "v_exp_f32 v172, v172\n\t" \
  "v_exp_f32 v173, v173\n\t" \
  "v_mfma_f32_32x32x16_bf16 v[150:165], v[222:225], v[210:213], 0\n\t" \
  "v_exp_f32 v134, v134\n\t" \
  "v_exp_f32 v135, v135\n\t" \
  "s_waitcnt vmcnt(5)\n\t" \
  "v_mfma_f32_16x16x32_bf16 v[18:21], v[14:17], v[122:125], v[18:21]\n\t" \
  "v_exp_f32 v136, v136\n\t" \
  "v_exp_f32 v137, v137\n\t" \
  "v_mfma_f32_32x32x8_bf16 v[150:165], v[232:233], v[218:219], v[150:165]\n\t" \
  "v_exp_f32 v138, v138\n\t" \
  "v_exp_f32 v139, v139\n\t" \
  "v_exp_f32 v140, v140\n\t" \
  "v_exp_f32 v141, v141\n\t" \
  "v_smfmac_f32_32x32x32_bf16 v[190:205], v[246:249], v[166:173], v255\n\t" \
  "v_exp_f32 v126, v126\n\t" \
  "v_exp_f32 v127, v127\n\t" \
  "v_mfma_f32_16x16x32_bf16 v[22:25], v[12:15], v[122:125], v[22:25]\n\t" \
  "s_add_u32 %[t0], %[w9last], 0\n\t" \
  "buffer_load_dwordx4 v[122:125], v10, %[srd], %[t0] offen sc1\n\t" \
  "v_exp_f32 v128, v128\n\t" \
  "v_exp_f32 v129, v129\n\t" \
  "v_smfmac_f32_32x32x32_bf16 v[174:189], v[234:237], v[134:141], v255\n\t" \
  "s_waitcnt lgkmcnt(0)\n\t" \
  "s_setprio 0\n\t" \
  "v_exp_f32 v130, v130\n\t" \
  "v_exp_f32 v131, v131\n\t" \
  "v_exp_f32 v132, v132\n\t" \
  "v_exp_f32 v133, v133\n\t" \
  "v_mfma_f32_32x32x16_bf16 v[134:149], v[226:229], v[206:209], 0\n\t" \
  "v_exp_f32 v158, v158\n\t" \
  "v_exp_f32 v159, v159\n\t" \
  "v_exp_f32 v160, v160\n\t" \
  "v_exp_f32 v161, v161\n\t" \
  "v_mfma_f32_32x32x8_bf16 v[134:149], v[232:233], v[216:217], v[134:149]\n\t" \
  "v_exp_f32 v162, v162\n\t" \
  "v_exp_f32 v163, v163\n\t" \
  "v_exp_f32 v164, v164\n\t" \
  "v_exp_f32 v165, v165\n\t" \
  "v_smfmac_f32_32x32x32_bf16 v[174:189], v[238:241], v[126:133], v255\n\t" \
  "ds_read_b128 v[246:249], v252 offset:3488\n\t" \
  "v_exp_f32 v150, v150\n\t" \
  "v_exp_f32 v151, v151\n\t" \
  "s_waitcnt vmcnt(5)\n\t" \
  "v_mfma_f32_16x16x32_bf16 v[26:29], v[14:17], v[38:41], 0\n\t" \
  "v_exp_f32 v152, v152\n\t" \
  "v_exp_f32 v153, v153\n\t" \
  "v_smfmac_f32_32x32x32_bf16 v[190:205], v[234:237], v[158:165], v255\n\t" \
  "v_exp_f32 v154, v154\n\t" \
  "v_exp_f32 v155, v155\n\t" \
  "v_mfma_f32_16x16x32_bf16 v[30:33], v[12:15], v[38:41], 0\n\t" \
  "v_exp_f32 v156, v156\n\t" \
  "v_exp_f32 v157, v157\n\t" \
  "v_mfma_f32_32x32x16_bf16 v[158:173], v[226:229], v[210:213], 0\n\t" \
  "v_exp_f32 v134, v134\n\t" \
  "v_exp_f32 v135, v135\n\t" \
  "v_exp_f32 v136, v136\n\t" \
  "v_exp_f32 v137, v137\n\t" \
  "v_mfma_f32_32x32x8_bf16 v[158:173], v[232:233], v[220:221], v[158:173]\n\t" \
  "v_exp_f32 v138, v138\n\t" \
  "v_exp_f32 v139, v139\n\t" \
  "v_exp_f32 v140, v140\n\t" \
  "v_exp_f32 v141, v141\n\t" \
  "v_smfmac_f32_32x32x32_bf16 v[190:205], v[238:241], v[150:157], v255\n\t" \
  "v_exp_f32 v142, v142\n\t" \
  "v_exp_f32 v143, v143\n\t" \
  "v_exp_f32 v144, v144\n\t" \
  "v_exp_f32 v145, v145\n\t" \
  "v_smfmac_f32_32x32x32_bf16 v[174:189], v[242:245], v[134:141], v255\n\t" \
  "s_waitcnt lgkmcnt(0)\n\t" \
  "v_exp_f32 v146, v146\n\t" \
  "v_exp_f32 v147, v147\n\t" \
  "v_exp_f32 v148, v148\n\t" \
  "v_exp_f32 v149, v149\n\t" \
  "v_exp_f32 v158, v158\n\t" \
  "v_exp_f32 v159, v159\n\t" \
  "s_waitcnt vmcnt(3)\n\t" \
  "v_mfma_f32_16x16x32_bf16 v[26:29], v[14:17], v[34:37], v[26:29]\n\t" \
  "v_exp_f32 v160, v160\n\t" \
  "v_exp_f32 v161, v161\n\t" \
  "v_exp_f32 v162, v162\n\t" \
  "v_exp_f32 v163, v163\n\t" \
  "v_exp_f32 v164, v164\n\t" \
  "v_exp_f32 v165, v165\n\t" \
  "v_smfmac_f32_32x32x32_bf16 v[174:189], v[246:249], v[142:149], v255\n\t" \
  "v_exp_f32 v166, v166\n\t" \
  "v_exp_f32 v167, v167\n\t" \
  "v_mfma_f32_16x16x32_bf16 v[30:33], v[12:15], v[34:37], v[30:33]\n\t" \
  "v_exp_f32 v168, v168\n\t" \
  "v_exp_f32 v169, v169\n\t" \
  "v_smfmac_f32_32x32x32_bf16 v[190:205], v[242:245], v[158:165], v255\n\t" \
  "v_exp_f32 v170, v170\n\t" \
  "v_exp_f32 v171, v171\n\t" \
  "v_exp_f32 v172, v172\n\t" \
  "v_exp_f32 v173, v173\n\t" \
  "s_nop 1\n\t" \
  "v_smfmac_f32_32x32x32_bf16 v[190:205], v[246:249], v[166:173], v255\n\t" \
  "v_add_u32 v250, %[xw], v10\n\t" \
  "v_add_u32 v251, %[xr], v10\n\t" \
  "v_mul_f32 v138, 0x5d800000, v184\n\t" \
  "v_add_f32 v138, v138, |v174|\n\t" \
  "v_add_f32 v138, v138, |v175|\n\t" \
  "v_add_f32 v138, v138, |v176|\n\t" \
  "v_add_f32 v138, v138, |v177|\n\t" \
  "v_add_f32 v138, v138, |v178|\n\t" \
  "v_add_f32 v138, v138, |v179|\n\t" \
  "v_add_f32 v138, v138, |v180|\n\t" \
  "v_add_f32 v138, v138, |v181|\n\t" \
  "v_add_f32 v138, v138, |v182|\n\t" \
  "v_add_f32 v138, v138, |v183|\n\t" \
  "ds_write_b128 v250, v[190:193] offset:0\n\t" \
  "ds_write_b128 v250, v[194:197] offset:1024\n\t" \
  "ds_write_b128 v250, v[198:201] offset:2048\n\t" \
  "v_mul_f32 v139, 0x5d800000, v200\n\t" \
  "v_add_f32 v139, v139, |v190|\n\t" \
  "v_add_f32 v139, v139, |v191|\n\t" \
  "v_add_f32 v139, v139, |v192|\n\t" \
  "v_add_f32 v139, v139, |v193|\n\t" \
  "v_add_f32 v139, v139, |v194|\n\t" \
  "v_add_f32 v139, v139, |v195|\n\t" \
  "v_add_f32 v139, v139, |v196|\n\t" \
  "v_add_f32 v139, v139, |v197|\n\t" \
  "v_add_f32 v139, v139, |v198|\n\t" \
  "v_add_f32 v139, v139, |v199|\n\t" \
  "v_add_f32 v138, v138, v139\n\t" \
  "v_cmp_ngt_f32 vcc, 0x7b800000, v138\n\t" \
  "v_mov_b32 v140, %[flw]\n\t" \
  "s_cmp_lg_u64 vcc, 0\n\t" \
  "s_cselect_b32 %[t0], 1, 0\n\t" \
  "v_mov_b32 v139, %[t0]\n\t" \
  "ds_write_b32 v140, v139\n\t" \
  "v_bfe_u32 v140, v10, 4, 3\n\t" \
  "v_lshl_add_u32 v140, v140, 2, %[flr]\n\t" \
  "v_bfe_u32 v254, v10, 8, 1\n\t" \
  "v_lshl_add_u32 v253, v254, 2, v11\n\t" \
  "v_lshrrev_b32 v252, 9, v10\n\t" \
  "v_lshl_or_b32 v253, v252, 30, v253\n\t" \
  "v_lshlrev_b32 v252, 8, v252\n\t" \
  "v_lshl_add_u32 v252, v254, 10, v252\n\t" \
  "v_lshl_add_u32 v252, v11, 1, v252\n\t" \
  "s_waitcnt lgkmcnt(0)\n\t" \
  "s_barrier\n\t" \
  "ds_read_b32 v140, v140\n\t" \
  "ds_read_b128 v[134:137], v251 offset:2048\n\t" \
  "ds_read_b128 v[126:129], v251 offset:0\n\t" \
  "ds_read_b128 v[130:133], v251 offset:1024\n\t" \
  "s_mov_b32 %[t0], 1\n\t" \
  "s_waitcnt lgkmcnt(2)\n\t" \
  "v_cmp_ne_u32 vcc, 0, v140\n\t" \
  "s_cbranch_vccnz 4f\n\t" \
  "s_mov_b32 %[t0], 0\n\t" \
  "v_add_f32 v142, v136, v184\n\t" \
  "v_pk_add_f32 v[134:135], v[134:135], v[182:183]\n\t" \
  "v_mov_b32 v143, v142\n\t" \
  "s_waitcnt lgkmcnt(0)\n\t" \
  "v_pk_add_f32 v[126:127], v[126:127], v[174:175]\n\t" \
  "v_permlane32_swap_b32 v142, v143\n\t" \
  "v_pk_add_f32 v[128:129], v[128:129], v[176:177]\n\t" \
  "v_rcp_f32 v142, v142\n\t" \
  "v_pk_add_f32 v[130:131], v[130:131], v[178:179]\n\t" \
  "v_pk_add_f32 v[132:133], v[132:133], v[180:181]\n\t" \
  "v_pk_mul_f32 v[134:135], v[134:135], v[142:143] op_sel_hi:[1,0]\n\t" \
  "v_pk_mul_f32 v[126:127], v[126:127], v[142:143] op_sel_hi:[1,0]\n\t" \
  "v_pk_mul_f32 v[128:129], v[128:129], v[142:143] op_sel_hi:[1,0]\n\t" \
  "v_pk_mul_f32 v[130:131], v[130:131], v[142:143] op_sel_hi:[1,0]\n\t" \
  "v_pk_mul_f32 v[132:133], v[132:133], v[142:143] op_sel_hi:[1,0]\n\t" \
  "v_cvt_pk_bf16_f32 v134, v134, v135\n\t" \
  "v_min_u32 v134, -2, v134\n\t" \
  "buffer_store_dword v134, v253, %[srd], %[osl] offen sc1\n\t" \
  "v_cvt_pk_bf16_f32 v126, v126, v127\n\t" \
  "v_cvt_pk_bf16_f32 v127, v128, v129\n\t" \
  "v_cvt_pk_bf16_f32 v128, v130, v131\n\t" \
  "v_cvt_pk_bf16_f32 v129, v132, v133\n\t" \
  "v_min_u32 v126, -2, v126\n\t" \
  "v_min_u32 v127, -2, v127\n\t" \
  "v_min_u32 v128, -2, v128\n\t" \
  "v_min_u32 v129, -2, v129\n\t" \
  "buffer_store_dwordx4 v[126:129], v252, %[srd], %[osm] offen sc1\n\t" \
  "4:\n\t" \
  "s_waitcnt lgkmcnt(0)\n\t" \
  "s_waitcnt vmcnt(0)\n\t" \
  ""
#define ATT_W_O "=&{v[174:205]}"
#define ATT_W_WF0 "+{v[42:73]}"
#define ATT_W_WF1 "+{v[74:105]}"
#define ATT_W_WF2 "=&{v[106:121]}"
#define ATT_W_WF3 "=&{v[122:125]}"
#define ATT_W_BS "=&{v[18:33]}"
#define ATT_R_E "{v[10:17]}"
#define ATT_W_GG "=&{v[8:9]}"
#define ATT_R_VV "{v6}"
#define ATT_RELOAD_ASM "s_add_u32 %[t0], %[wsrc], 0\n\tbuffer_load_dwordx4 v[42:45], %[vo], %[srd], %[t0] offen sc1\n\ts_add_u32 %[t0], %[wsrc], 1024\n\tbuffer_load_dwordx4 v[46:49], %[vo], %[srd], %[t0] offen sc1\n\ts_add_u32 %[t0], %[wsrc], 2048\n\tbuffer_load_dwordx4 v[50:53], %[vo], %[srd], %[t0] offen sc1\n\ts_add_u32 %[t0], %[wsrc], 3072\n\tbuffer_load_dwordx4 v[54:57], %[vo], %[srd], %[t0] offen sc1\n\ts_add_u32 %[t0], %[wsrc], 4096\n\tbuffer_load_dwordx4 v[58:61], %[vo], %[srd], %[t0] offen sc1\n\ts_add_u32 %[t0], %[wsrc], 5120\n\tbuffer_load_dwordx4 v[62:65], %[vo], %[srd], %[t0] offen sc1\n\ts_add_u32 %[t0], %[wsrc], 6144\n\tbuffer_load_dwordx4 v[66:69], %[vo], %[srd], %[t0] offen sc1\n\ts_add_u32 %[t0], %[wsrc], 7168\n\tbuffer_load_dwordx4 v[70:73], %[vo], %[srd], %[t0] offen sc1\n\ts_add_u32 %[t0], %[wsrc], 8192\n\tbuffer_load_dwordx4 v[74:77], %[vo], %[srd], %[t0] offen sc1\n\ts_add_u32 %[t0], %[wsrc], 9216\n\tbuffer_load_dwordx4 v[78:81], %[vo], %[srd], %[t0] offen sc1\n\ts_add_u32 %[t0], %[wsrc], 10240\n\tbuffer_load_dwordx4 v[82:85], %[vo], %[srd], %[t0] offen sc1\n\ts_add_u32 %[t0], %[wsrc], 11264\n\tbuffer_load_dwordx4 v[86:89], %[vo], %[srd], %[t0] offen sc1\n\ts_add_u32 %[t0], %[wsrc], 12288\n\tbuffer_load_dwordx4 v[90:93], %[vo], %[srd], %[t0] offen sc1\n\ts_add_u32 %[t0], %[wsrc], 13312\n\tbuffer_load_dwordx4 v[94:97], %[vo], %[srd], %[t0] offen sc1\n\ts_add_u32 %[t0], %[wsrc], 14336\n\tbuffer_load_dwordx4 v[98:101], %[vo], %[srd], %[t0] offen sc1\n\ts_add_u32 %[t0], %[wsrc], 15360\n\tbuffer_load_dwordx4 v[102:105], %[vo], %[srd], %[t0] offen sc1\n\ts_add_u32 %[t0], %[wsrc], 16384\n\tbuffer_load_dwordx4 v[106:109], %[vo], %[srd], %[t0] offen sc1\n\ts_add_u32 %[t0], %[wsrc], 17408\n\tbuffer_load_dwordx4 v[110:113], %[vo], %[srd], %[t0] offen sc1\n\ts_add_u32 %[t0], %[w9src], 0\n\tbuffer_load_dwordx4 v[114:117], %[vo], %[srd], %[t0] offen sc1\n\ts_add_u32 %[t0], %[w9src], 8192\n\tbuffer_load_dwordx4 v[118:121], %[vo], %[srd], %[t0] offen sc1\n\ts_add_u32 %[t0], %[w9last], 0\n\tbuffer_load_dwordx4 v[122:125], %[vo], %[srd], %[t0] offen sc1\n\ts_waitcnt vmcnt(0)"
#define ATT_O_WF0 "=&{v[42:73]}"
#define ATT_O_WF1 "=&{v[74:105]}"
#define ATT_O_CLOB "v174", "v175", "v176", "v177", "v178", "v179", "v180", "v181", "v182", "v183", "v184", "v185", "v186", "v187", "v188", "v189", "v190", "v191", "v192", "v193", "v194", "v195", "v196", "v197", "v198", "v199", "v200", "v201", "v202", "v203", "v204", "v205"
#define ATT_NC 8
#define ATT_FULL_CLOB "v34", "v35", "v36", "v37", "v38", "v39", "v40", "v41"
#define ATT_SEG_CLOB "v126", "v127", "v128", "v129", "v130", "v131", "v132", "v133", "v134", "v135", "v136", "v137", "v138", "v139", "v140", "v141", "v142", "v143", "v144", "v145", "v146", "v147", "v148", "v149", "v150", "v151", "v152", "v153", "v154", "v155", "v156", "v157", "v158", "v159", "v160", "v161", "v162", "v163", "v164", "v165", "v166", "v167", "v168", "v169", "v170", "v171", "v172", "v173", "v222", "v223", "v224", "v225", "v226", "v227", "v228", "v229", "v230", "v231", "v232", "v233", "v234", "v235", "v236", "v237", "v238", "v239", "v240", "v241", "v242", "v243", "v244", "v245", "v246", "v247", "v248", "v249"
constexpr int d_i(int n) { return n < 12 ? n / 6 : n < 16 ? (n - 12) / 2 : n < 18 ? n - 16 : n / 9; }
constexpr int d_k(int n) { return n < 12 ? n % 6 : n < 16 ? 6 + (n & 1) : n < 18 ? 8 : n % 9; }
constexpr int d_sti(int n) { return n == 18 || n == 20 ? 0 : n == 22 || n == 24 ? 1 : (n >= 27 && (n % 9 == 1 || n % 9 == 5)) ? n / 9 - 1 : -1; }
constexpr int d_stp(int n) { return n < 27 ? ((n - 18) >> 1) & 1 : (n % 9) >> 2; }
constexpr int d_ring(int n) { return ((n & 1) && n <= 65) ? 1 : 0; }
constexpr int d_kv(int n) { return (n >= 16 && n < 56 && (n & 3) == 2) ? 1 : 0; }
constexpr int d_qi(int n) { return n == 44 ? 0 : n == 48 ? 1 : n == 52 ? 2 : n == 56 ? 3 : n == 58 ? 4 : n == 60 ? 5 : -1; }
constexpr int d_q(int n) { return d_qi(n) >= 0 ? 1 : 0; }
constexpr int d_stm(int n) { constexpr int NS = 18 - ATT_NC; for (int m = 0; m < NS; ++m) if (n == 20 + (m * 56) / NS) return m; return -1; }
constexpr int d_st(int n) { return d_stm(n) >= 0 ? 1 : 0; }
constexpr int d_cnt(int s) { const int n0 = s == 19 ? 65 : 4 * (s - 3) + 3; int c = n0 >= 0 ? d_st(n0) : 0; for (int n = n0 + 1; n <= 4 * s; ++n) c += d_ring(n) + (n >= 0 ? d_kv(n) + d_q(n) + d_st(n) : 0); return c; }
constexpr bool s_isq(int sg) { return sg < 12 ? !(sg & 1) : sg < 15; }
constexpr int s_slice(int sg) { return sg < 12 ? sg >> 1 : sg < 15 ? sg - 6 : sg - 9; }
constexpr int s_xr(int i) { return (7 * i + 2) / 4; }
constexpr int s_xat(int sg) { for (int i = 0; i < 9; ++i) if (s_xr(i) == sg) return i; return -1; }
constexpr int s_posw(int sg) { return sg == 0 ? -6 : sg == 1 ? -3 : sg == 2 ? 1 : sg == 3 ? 3 : 8 * (sg - 4) + 7; }
constexpr int s_posx(int i) { return i == 0 ? -5 : i == 1 ? -1 : 8 * (s_xr(i) - 3) + 3; }
constexpr int s_epi(int F) { return F == 123 ? 0 : F == 129 ? 1 : F == 135 ? 2 : F == 141 ? 3 : -1; }
constexpr int s_sto(int F) { for (int k = 0; k < 4; ++k) if (F == 154 + 16 * k) return k; return -1; }
constexpr int s_ops(int F) { if (F < 4) return 1; const int f = F & 7; if (f == 5 || f == 7) return 1; if ((f == 1 || f == 3) && s_xat((F >> 3) + 3) >= 2) return 1; return s_sto(F) >= 0 ? 1 : F == 218 ? 2 : 0; }
constexpr int s_cnt(int sg) { int last = s_posw(sg + 1); const int xi = s_xat(sg + 1); if (xi >= 0 && s_posx(xi) > last) last = s_posx(xi); int c = 0; for (int F = last + 1; F < 8 * sg + 5; ++F) c += s_ops(F); return c; }
template <int V> using IC = std::integral_constant<int, V>;

__global__ __launch_bounds__(512, 2) void k_all(const char* __restrict__ x, const char* __restrict__ wqkv, const char* __restrict__ wo, const char* __restrict__ wg,
                                                const char* __restrict__ wu, const char* __restrict__ wd, const char* __restrict__ gw, char* ws, char* out) {
  extern __shared__ __attribute__((aligned(16))) char smem[];
  const lds_ptr lds = (lds_ptr)smem; const unsigned lds0 = (unsigned)(uintptr_t)lds;
  const int b = blockIdx.x & 255, xc = b & 7, rr_ = b >> 3, j = 2 * xc + (rr_ & 1), tg = rr_ >> 1;
  int lane = threadIdx.x & 63; const int w = __builtin_amdgcn_readfirstlane(threadIdx.x >> 6), t = w & 3, hh = w >> 2;
  int c = lane & 15, q = lane >> 4;
  const int rot = tg >= 9 ? tg - 9 : tg;
  const u32x4 srd_x = make_srd(x, S_ * D_ * 2), srd_w = make_srd(hh ? wu : wg, D_ * F_ * 2), srd_d = make_srd(wd, F_ * D_ * 2);
  const u32x4 srd_a = j < 14 ? make_srd(wqkv, D_ * 3 * D_ * 2) : make_srd(wo, D_ * D_ * 2);
  const u32x4 srd_ws = make_srd(ws, WS_END);
  const __amdgpu_buffer_rsrc_t rs_ws = __builtin_amdgcn_make_buffer_rsrc(ws, 0, WS_END, 0x20000);
  const int pi = lane >> 3, pc = lane & 7;
  const unsigned krow = 8 * (pi & 1) + 16 * (pi >> 2) + 4 * ((pi >> 1) & 1) + t;
  const unsigned xr = 8 * w + pi, vo_x = (128 * tg + (xr & 32) + 2 * (xr & 15) + ((xr >> 4) & 1)) * 1152 + ((pc ^ ((xr >> 1) & 7)) * 16);
  const unsigned vo_qkv = krow * 3456 + pc * 16 + (j < 13 ? 256 * j : 3200) + hh * 128, vo_w = krow * 9216 + pc * 16 + (9 * (j >> 1) + ((j & 1) ? 5 : 0)) * 128;
  const int colbase = j < 13 ? 128 * j : 1600;
  const int fullline = 9 * (j >> 1) + ((j & 1) ? 5 : 0), halfline = 9 * (j >> 1) + 4;
  const unsigned dst_w = lds0 + w * PIECE;
  int ksl[10];
#pragma unroll
  for (int i = 0; i < 9; ++i) { const int v = i + rot; ksl[i] = v >= 9 ? v - 9 : v; }
  ksl[9] = 0;
  auto kslice = [&](int i) { return ksl[i]; };
  const unsigned dst_x = lds0 + w * 1024;
  auto issue_x = [&](int i, int h) { dma16i(srd_x, vo_x, kslice(i) * 128, h * 64 * 1152, dst_x, L_H + (i & 3) * 16384 + h * 8192); };
  auto issue_w = [&](int r, int i, int h, int so) { dma16i(srd_w, vo_w, kslice(i) * 64 * 9216, 32 * h * 9216 + r * 128, dst_w, so + h * 8 * PIECE); };
  const int en = 2 * tg + (j - 14);
  auto issue_a = [&](auto mode_, int i, int h, int so) {
    if constexpr (decltype(mode_)::value < 2) dma16i(srd_a, vo_qkv, kslice(i) * 64 * 3456, 32 * h * 3456, dst_w, so + h * 8 * PIECE);
    else if (i >= 2) dma16i(srd_a, vo_qkv, dst_w, OOB, dst_w, so + h * 8 * PIECE);
    else { const int n = en + 32 * (4 * i + 2 * h + hh), kap = (n * 57) >> 9, ln = n - 9 * kap, qq = krow >> 3, jj = krow & 7;
      const int rowm = 18 * (2 * kap + (qq >> 1)) + 4 * (qq & 1) + (jj & 3) + 8 * (jj >> 2), rowl = 18 * (16 * (kap - 16) + 4 * qq + (jj >> 1)) + 16 + (jj & 1);
      dma16(srd_a, (kap < 16 ? rowm : rowl) * 1152 + pc * 16, n < 162 ? ln * 128 : OOB, dst_w + so + h * 8 * PIECE); } };
  const int trb = ((lane >> 2) & 3) * PIECE + ((q & 1) + 4 * (q >> 1)) * 128 + (lane & 3) * 8;
  int so = 0, po = 4 * SLOT;
  auto nextso = [&](int v) { return v == (NSLOT - 1) * SLOT ? 0 : v + SLOT; };

  if (j == 15 && w < 2) dma16(make_srd(gw, D_ * 2), lane * 16, w * 1024, lds0 + L_GW + w * 1024);
  unsigned ep;
  auto prologue = [&](auto mode_) { issue_a(mode_, 0, 0, 0); issue_x(0, 0); issue_a(mode_, 0, 1, 0); issue_x(0, 1); };
  if (j < 14) prologue(IC<0>{}); else prologue(IC<2>{});
  unsigned pf0 = 0;
  if (w == 0) {
    uint64_t pc_; unsigned len_;
    asm volatile("s_getpc_b64 %0\nL_code_begin:\n\ts_mov_b32 %1, L_code_end-L_code_begin" : "=s"(pc_), "=s"(len_));
    asm volatile("buffer_load_dword %0, %1, %2, 0 offen" : "+v"(pf0) : "v"(lane < 16 ? (lane * 32 + rr_) * 128 : OOB), "s"(make_srd((const void*)pc_, len_)) : "memory"); }
  asm volatile("buffer_load_dword %0, off, %1, 0 sc1" : "=v"(ep) : "s"(srd_ws) : "memory");
  issue_w(0, 0, 0, SLOT); issue_w(0, 0, 1, SLOT);
  issue_x(1, 0); issue_x(1, 1);
  s16x8 xf[18][2];
  f32x4 accA[2][2][2], accB[2][2][2];
  s16x8 a[4];
  const f32x4 z4 = {0.f, 0.f, 0.f, 0.f};
  asm volatile("s_waitcnt vmcnt(5)\n\ts_barrier" : "+v"(pf0) :: "memory");
  lds_ptr pA = lds + trb + hh * 64, pB = pA + 3 * SLOT; PIN(pA); PIN(pB);
  auto rebase = [&](int base) { pA = lds + base; pB = pA + 3 * SLOT; PIN(pA); PIN(pB); };
  auto ringp = [&]() { return so < 3 * SLOT ? pA + so : pB + (so - 3 * SLOT); };
  lds_ptr p = pA;
  const int head = 4 * xc + (rr_ & 3), qb = rr_ >> 2, odd = head & 1;
  auto qkv_epi = [&](auto mode_, int L, int u) {
    const int col0 = colbase + 64 * L + 32 * hh + 16 * u;
    const f32x4 v0 = accA[L][u][0], v1 = accA[L][u][1]; u32x4 o;
    if constexpr (decltype(mode_)::value == 0) {
      const float sc = col0 >= 576 ? 1.0f : 0.23570226039551584f * 1.4426950408889634f;
      o = (u32x4){cvtpk(fmul1(v0[0], sc), fmul1(v0[1], sc)), cvtpk(fmul1(v0[2], sc), fmul1(v0[3], sc)), cvtpk(fmul1(v1[0], sc), fmul1(v1[1], sc)), cvtpk(fmul1(v1[2], sc), fmul1(v1[3], sc))};
    } else o = (u32x4){cvtpk(v0[0], v1[0]), cvtpk(v0[1], v1[1]), cvtpk(v0[2], v1[2]), cvtpk(v0[3], v1[3])};
    *(LAS u32x4*)(lds + L_H + 32768 + (4 * w + 2 * L + u) * 1024 + (decltype(mode_)::value == 0 ? lane * 16 : c * 64 + q * 16)) = o; };
  u32x4 qs;
  auto qkv_get = [&](int k) { qs = *(const LAS u32x4*)(lds + L_H + 32768 + (4 * w + k) * 1024 + fresh_lane() * 16); };
  const int cw0 = colbase + 32 * hh - 1152;
  unsigned qa0 = j < 9 ? 0 : cw0, qa1 = j < 9 ? 4 : 2, qa2 = j < 9 ? 65536 : 3641, qa3 = j < 9 ? 16384 - 256 : 73728 - 1152, qa4 = j < 9 ? 0 : 16, qa5 = j < 9 ? 65536 : 1024;
  unsigned qb0 = j < 9 ? WS_QK + ((colbase + 32 * hh) >> 2) * 16384 + (128 * tg + 32 * t) * 8 : j > 13 ? OOB : WS_V + (4 * tg + t) * 1152 + cw0 * 64, qb1 = j == 13 ? OOB : qb0;
  auto qkv_put = [&](int k) { const unsigned ln = fresh_lane(); const int kk = 4 * (k >> 1) + (k & 1);
    if (k == 0) asm volatile("" : "+s"(qa0), "+s"(qa1), "+s"(qa2), "+s"(qa3), "+s"(qa4), "+s"(qa5), "+s"(qb0), "+s"(qb1));
    const unsigned ix = __umul24(qa0 + kk * qa4 + (ln >> qa1), qa2) >> 16;
    __builtin_amdgcn_raw_buffer_store_b128(qs, rs_ws, __umul24(ix, qa3) + ln * 16, (k < 2 ? qb1 : qb0) + kk * qa5, SC1);
    SBAR(); asm volatile("s_nop 1"); };
  unsigned ov_m = 0, ov_l = 0, os_m = 0, os_l = 0;
  auto o_addr = [&]() { const int ln = fresh_lane(); const unsigned ob = WS_O + (8 * qb + 2 * t + hh) * 36 * 1024;
    ov_m = ((ln >> 4) & 1) * 1024 + (ln & 15) * 16 + (ln >> 5) * 256; ov_l = ln < 32 ? (ln & 15) * 8 + (ln >> 4) * 4 : OOB;
    os_m = ob + (head >> 1) * 2048 + odd * 512; os_l = ob + 32768 + head * 128; asm volatile("" : "+v"(ov_m), "+v"(ov_l), "+s"(os_m), "+s"(os_l)); };
  auto store_o = [&](u32x4 om, unsigned ol) {
    __builtin_amdgcn_raw_buffer_store_b32(ol, rs_ws, ov_l, os_l, SC1);
    SBAR();
    __builtin_amdgcn_raw_buffer_store_b128(om, rs_ws, ov_m, os_m, SC1);
    SBAR(); asm volatile("s_nop 1"); };
  {
    lds_ptr xr0 = lds + L_H + (32 * t + c) * 128 + ((q ^ ((c >> 1) & 7)) * 16), xr1 = lds + L_H + (32 * t + c) * 128 + (((4 + q) ^ ((c >> 1) & 7)) * 16); PIN(xr0); PIN(xr1);
    auto rdx = [&](int i, int k) { xf[2 * i + (k >> 1)][k & 1] = ldsr128(((k >> 1) ? xr1 : xr0) + (i & 3) * 16384 + (k & 1) * 2048); };
    auto first_pass = [&](auto mode_) { constexpr int MODE = decltype(mode_)::value;
      auto issue_s = [&](int sg, int h) { const int fo = (sg % 5) * SLOT;
        if (sg >= 18) issue_w(1, sg - 18, h, fo); else if (s_isq(sg)) issue_a(mode_, s_slice(sg), h, fo); else issue_w(0, s_slice(sg), h, fo); };
#pragma unroll
      for (int k = 0; k < 4; ++k) rdx(0, k);
      a[0] = trfrag(p); a[1] = trfrag(p + 32); a[2] = trfrag(p + KC);
      sfor<0, 144>([&](auto F_) { constexpr int F = decltype(F_)::value, sg = F >> 3, i = s_slice(sg), f = F & 7, L = (f >> 1) & 1, u = f & 1, ks = 2 * i + (f >> 2), g = f + 3;
        constexpr bool Q = s_isq(sg), Z = i == 0 && f < 4;
        if constexpr (MODE == 2 && Q && i < 2 && (f == 0 || f == 2)) { const int n = en + 32 * (4 * i + f + hh), kap = (n * 57) >> 9, ln = n - 9 * kap;
          const s16x8 fr = trfrag(lds + trb + so + (f + hh) * KC + t * 32);
          __builtin_amdgcn_raw_buffer_store_b128(__builtin_bit_cast(u32x4, fr), rs_ws, lane * 16, n < 162 ? WS_WO + ((4 * ln + t) * 18 + kap) * 1024 : OOB, SC1); SBAR(); asm volatile("s_nop 1"); SBAR(); }
        if constexpr (g == 8) { po = so; so = nextso(so); p = ringp(); SBAR();
          if constexpr (MODE == 2 && Q) lgkm<0>();
          vm_bar<s_cnt(sg)>(); }
#define MMX(ts) do { \
          if constexpr (!Q) { if constexpr (Z) MF0(accB[L][u][ts], a[F & 3], xf[ks][ts]); else MF(accB[L][u][ts], a[F & 3], xf[ks][ts]); } \
          if constexpr (Q && MODE == 0) { if constexpr (Z) MF0(accA[L][u][ts], a[F & 3], xf[ks][ts]); else MF(accA[L][u][ts], a[F & 3], xf[ks][ts]); } \
          if constexpr (Q && MODE == 1) { if constexpr (Z) MF0(accA[L][u][ts], xf[ks][ts], a[F & 3]); else MF(accA[L][u][ts], xf[ks][ts], a[F & 3]); } } while (0)
        MMX(0); SBAR();
        if constexpr (MODE < 2 || !s_isq((F + 3) >> 3)) a[(F + 3) & 3] = trfrag(p + ((g & 7) >> 1) * KC + (g & 1) * 32);
        SBAR();
        MMX(1); SBAR();
        if constexpr (f & 1) { lgkm<2>(); SBAR(); }
        constexpr int xi = s_xat(sg + 1);
        if constexpr (xi > 0 && f == 5) { rdx(xi, 0); rdx(xi, 1); }
        if constexpr (xi > 0 && f == 7) { rdx(xi, 2); rdx(xi, 3); }
        if constexpr (F < 4) issue_s(2 + (F >> 1), F & 1);
        else if constexpr (f == 5 || f == 7) issue_s(sg + 4, f == 7);
        else if constexpr ((f == 1 || f == 3) && s_xat(sg + 3) >= 2) issue_x(s_xat(sg + 3), f == 3);
        if constexpr (s_epi(F) >= 0 && MODE < 2) qkv_epi(mode_, s_epi(F) >> 1, s_epi(F) & 1);
        SBAR();
      });
    };
    if (j < 9) first_pass(IC<0>{}); else if (j < 14) first_pass(IC<1>{}); else first_pass(IC<2>{});
    asm volatile("" : "+v"(ep));
    if (j == 15 && hh == 0) {
      f32x4 g0 = z4, g1 = z4;
#pragma unroll
      for (int ks = 0; ks < 18; ++ks) {
        const s16x8 gf = ldsr128(lds + L_GW + (kslice(ks >> 1) * 64 + (ks & 1) * 32 + 8 * q) * 2);
        g0 = mfma16(gf, xf[ks][0], g0); g1 = mfma16(gf, xf[ks][1], g1);
      }
      const float ga = __builtin_amdgcn_rcpf(1.0f + __builtin_amdgcn_exp2f(-1.4426950408889634f * g0[0]));
      const float gb = __builtin_amdgcn_rcpf(1.0f + __builtin_amdgcn_exp2f(-1.4426950408889634f * g1[0]));
      const u32x2 o = {__float_as_uint(ga), __float_as_uint(gb)};
      if (q == 0) __builtin_amdgcn_raw_buffer_store_b64(o, rs_ws, c * 8, WS_G + (128 * tg + 32 * t) * 4, SC1);
    }
  }
  const unsigned tag = (unsigned)__builtin_amdgcn_readfirstlane(ep) + 0x9E3779B9u;
  unsigned vo_d = 0, vo_h = 0; int trh = 0;
  unsigned kd[10];
  const unsigned d8 = ((j & 1) ? -9 : 0) * 32 * 1152;
  auto issue_d = [&](int s, int h, int fo) { const int n = 4 * s + 2 * h, i = d_i(n), kp = d_k(n);
    const unsigned sb = kp < 7 ? kd[i] : kp == 7 ? kd[i] + (hh ? d8 : 0) : n == 16 ? d8 + (hh ? kd[1] - 32 * 1152 : kd[0]) : hh ? (n == 80 ? OOB : kd[i + 1] - 9 * 32 * 1152) : kd[i] + d8;
    dma16i(srd_d, vo_d, sb, kp * 32 * 1152, dst_w, fo + h * 8 * PIECE); };
  auto issue_h = [&](int i, int h, int fo) { if (2 * i + h < 9) dma16i(srd_w, vo_h, ksl[2 * i + h] * 64 * 9216, 0, lds0 + (4 * (t >> 1) + 2 * hh + (t & 1)) * PIECE, fo + 8 * h * PIECE);
    else dma16i(srd_w, vo_h, dst_w, OOB, dst_w, fo + 8 * h * PIECE); };
  u32x4 hp[2];
  constexpr float K1 = -2.0f * 0.7978845608028654f * 1.4426950408889634f, K2 = K1 * 0.044715f;
#define GEL(P, L, e) (((float*)&P[L][(e) >> 3][((e) >> 2) & 1])[(e) & 3])
#define G_CVT(P, k) asm volatile("v_cvt_pk_bf16_f32 %0, %1, %2" : "=v"(hp[((k) >> 1) & 1][2 * ((k) >> 2) + ((k) & 1)]) : "v"(GEL(P, 1, 2 * (k))), "v"(GEL(P, 1, 2 * (k) + 1)))
  f32x2 K1p = {K1, K1}, K2p = {K2, K2}; PIN(K1p); PIN(K2p);
#define GP(P, L, e2) (((f32x2*)&P[L][(e2) >> 2][((e2) >> 1) & 1])[(e2) & 1])
#define G_BURST(P, n2) do { asm volatile("s_nop 7"); _Pragma("unroll") for (int e2 = 0; e2 < (n2); e2 += 2) { f32x2 t0_, t1_; \
      asm volatile("v_pk_mul_f32 %0, %2, %2\n\tv_pk_mul_f32 %1, %3, %3\n\tv_pk_mul_f32 %4, %4, %2\n\tv_pk_mul_f32 %5, %5, %3\n\tv_pk_fma_f32 %0, %0, %6, %7\n\tv_pk_fma_f32 %1, %1, %6, %7\n\tv_pk_mul_f32 %2, %2, %0\n\tv_pk_mul_f32 %3, %3, %1" \
                   : "=&v"(t0_), "=&v"(t1_), "+v"(GP(P, 0, e2)), "+v"(GP(P, 0, e2 + 1)), "+v"(GP(P, 1, e2)), "+v"(GP(P, 1, e2 + 1)) : "v"(K2p), "v"(K1p)); } SBAR(); } while (0)
#define B_EXP(P, e) asm volatile("v_exp_f32 %0, %0" : "+v"(GEL(P, 0, e)))
#define B_ADD(P, e) asm volatile("v_add_f32 %0, 1.0, %0" : "+v"(GEL(P, 0, e)))
#define B_RCP(P, e) asm volatile("v_rcp_f32 %0, %0" : "+v"(GEL(P, 0, e)))
#define B_FIN(P, e) asm volatile("v_mul_f32 %0, %0, %1" : "+v"(GEL(P, 1, e)) : "v"(GEL(P, 0, e)))
  auto ffn_round = [&](f32x4 (&C)[2][2][2], f32x4 (&P)[2][2][2], auto r_) { constexpr int r = decltype(r_)::value;
    constexpr int line = r, nline = r + 1;
    int fo = po;
    G_BURST(P, 8);
    sfor<0, 72>([&](auto F_) { constexpr int F = decltype(F_)::value, i = F >> 3, f = F & 7, L = (f >> 1) & 1, u = f & 1, ks = 2 * i + (f >> 2), g = f + 3;
      if constexpr (g == 8) { fo = po; po = so; so = nextso(so); if constexpr (r == 3 && i == 8) rebase(trh); p = ringp(); SBAR();
        vm_bar<(r == 2 && i == 4) ? 0 : s_cnt(9 + 9 * r + i)>(); }
      if constexpr (i == 0 && f < 4) MF0(C[L][u][0], a[F & 3], xf[ks][0]); else MF(C[L][u][0], a[F & 3], xf[ks][0]);
      SBAR();
      if constexpr (r == 3 && F + 3 >= 72) a[(F + 3) & 3] = trfrag(p + (g & 7) * 2 * PIECE); else a[(F + 3) & 3] = trfrag(p + ((g & 7) >> 1) * KC + (g & 1) * 32);
      SBAR();
      if constexpr (i == 0 && f < 4) MF0(C[L][u][1], a[F & 3], xf[ks][1]); else MF(C[L][u][1], a[F & 3], xf[ks][1]);
      if constexpr (F < 64) { constexpr int e = F >> 2; if constexpr ((F & 3) == 0) B_EXP(P, e); if constexpr ((F & 3) == 1) B_ADD(P, e); if constexpr ((F & 3) == 2) B_RCP(P, e); if constexpr ((F & 3) == 3) B_FIN(P, e); }
      else if constexpr (F < 70) { G_CVT(P, F - 64); if constexpr (F == 64 || F == 66) G_CVT(P, 6 + (F - 64) / 2); }
      SBAR();
      if constexpr (f & 1) { lgkm<2>(); SBAR(); }
      if constexpr (s_sto(72 + 72 * r + F + 3) >= 0) qkv_get(s_sto(72 + 72 * r + F + 3));
      if constexpr (s_sto(72 + 72 * r + F) >= 0) qkv_put(s_sto(72 + 72 * r + F));
      if constexpr (r == 2 && F == 2) { o_addr(); store_o((u32x4){~0u, ~0u, ~0u, ~0u}, ~0u); }
      if constexpr (r == 2 && F == 42) { if (w == 0) __builtin_amdgcn_raw_buffer_store_b32(tag, rs_ws, fresh_lane() == 0 ? 0u : OOB, WS_CTL + 1024 + b * 4, SC1); }
      if constexpr (r == 3 && F == 2) { const int ln = fresh_lane(), ri = ln >> 2;
        vo_h = (32 * (t >> 1) + 8 * (t & 1) + 16 * (ri >> 3) + (ri & 7)) * 9216 + (ln & 3) * 16 + halfline * 128 + (j & 1) * 64;
        trh = ((ln >> 4) & 1) * PIECE + ((ln >> 2) & 3) * 64 + (ln >> 5) * 512 + (ln & 3) * 8 + hh * 32; }
      if constexpr (F == 71) { const lds_ptr hw = lds + L_H + ((2 * (r - 1) + hh) * 8 + 2 * t) * 1024 + fresh_lane() * 16; *(LAS u32x4*)hw = hp[0]; *(LAS u32x4*)(hw + 1024) = hp[1]; }
      if constexpr (f == 5 || f == 7) { constexpr int h = f == 7;
        if constexpr (i + 4 <= 8) issue_w(line, i + 4, h, fo); else if constexpr (r < 3) issue_w(nline, i - 5, h, fo); else issue_h(i - 5, h, fo); }
      SBAR();
    });
  };
  ffn_round(accA, accB, IC<1>{}); ffn_round(accB, accA, IC<2>{}); ffn_round(accA, accB, IC<3>{});
#pragma unroll
  for (int kp = 0; kp < 9; ++kp) { asm volatile("" : "+s"(ksl[kp])); kd[kp] = ksl[kp] * 128 + (64 * fullline + 32 * hh) * 1152; }
  kd[9] = 0;
  lane = fresh_lane();
  { const int pi2 = lane >> 3; vo_d = (16 * ((pi2 >> 1) & 1) + 4 * (pi2 & 1) + 8 * (pi2 >> 2) + t) * 1152 + (lane & 7) * 16; }
  u32x4 fa; asm volatile("buffer_load_dwordx4 %0, %1, %2, 0 offen offset:1024 sc1" : "=v"(fa) : "v"(lane * 16), "s"(srd_ws) : "memory");
  s16x8 hf[9][4];
  lds_ptr hr = lds + L_H + 4 * hh * 1024 + lane * 16; PIN(hr);
  {
    int fo = po;
    G_BURST(accA, 8);
    sfor<0, 36>([&](auto H_) { constexpr int H = decltype(H_)::value, i = H >> 3, f = H & 7, L = H & 1, ks = H >> 1;
      if constexpr (f == 5 || H == 33) { fo = po; po = so; so = nextso(so); if constexpr (H == 33) rebase(trb + t * 32); p = ringp(); SBAR(); vm_bar<4>(); }
      if constexpr (H < 2) MF0(accB[L][0][0], a[H & 3], xf[ks][0]); else MF(accB[L][0][0], a[H & 3], xf[ks][0]);
      SBAR();
      if constexpr (H >= 33) a[(H + 3) & 3] = trfrag(p + (H - 33) * KC); else a[(H + 3) & 3] = trfrag(p + ((f + 3) & 7) * 2 * PIECE);
      SBAR();
      if constexpr (H >= 1 && H <= 32) { if constexpr (H & 1) B_ADD(accA, (H - 1) >> 1); else B_FIN(accA, (H - 2) >> 1); }
      if constexpr (H >= 33) { G_CVT(accA, H - 33); }
      if constexpr (H < 2) MF0(accB[L][0][1], a[H & 3], xf[ks][1]); else MF(accB[L][0][1], a[H & 3], xf[ks][1]);
      if constexpr (H < 32) { if constexpr (H & 1) B_RCP(accA, H >> 1); else B_EXP(accA, H >> 1); }
      if constexpr (H == 32 || H == 33) { G_CVT(accA, 3 + 2 * (H - 32)); G_CVT(accA, 4 + 2 * (H - 32)); }
      if constexpr (H == 34) G_CVT(accA, 7);
      SBAR();
      if constexpr (H & 1) { lgkm<2>(); SBAR(); }
      if constexpr ((H & 1) && H >= 7 && H < 31) { constexpr int m = H - 7; hf[m >> 2][m & 3] = ldsr128(hr + ((m >> 2) * 8 + (m & 3)) * 1024); hf[m >> 2][(m & 3) + 1] = ldsr128(hr + ((m >> 2) * 8 + (m & 3) + 1) * 1024); }
      if constexpr (H == 35) { const lds_ptr hw = lds + L_H + ((6 + hh) * 8 + 2 * t) * 1024 + lane * 16; *(LAS u32x4*)hw = hp[0]; *(LAS u32x4*)(hw + 1024) = hp[1]; }
      if constexpr (f == 5 || f == 7) { if constexpr (i == 0) issue_h(4, f == 7, fo); else issue_d(i - 1, f == 7, fo); }
      if constexpr (H == 33 || H == 35) issue_d(3, H == 35, fo);
      SBAR();
    });
    asm volatile("" : "+v"(fa) :: "memory");
  }
  for (unsigned spin = 0; !__all(fa[0] == tag && fa[1] == tag && fa[2] == tag && fa[3] == tag) && spin < (1u << 16); ++spin) {
    __builtin_amdgcn_s_sleep(2);
    asm volatile("buffer_load_dwordx4 %0, %1, %2, 0 offen offset:1024 sc1\n\ts_waitcnt vmcnt(0)" : "=&v"(fa) : "v"(lane * 16), "s"(srd_ws) : "memory"); }
  if (b == 0 && w == 0 && lane == 0) __builtin_amdgcn_raw_buffer_store_b32(tag, rs_ws, 0, WS_CTL, SC1);
  auto issue_kv = [&](int half, int k) {
    const int tgk = 8 * half + w;
    if (k < 5) dma16ic(srd_ws, lane * 16, (9 * (head >> 1) + 4 * odd) * 16384 + tgk * 1024, WS_QK + (144 + k) * 16384, lds0 + w * 5120, (half ? L_K1 : L_K0) + k * 1024);
    else dma16ic(srd_ws, lane * 16, head * 73728 + tgk * 4608, WS_V + (k - 5) * 1024, lds0 + w * 4608, (half ? L_V1 : L_V0) + (k - 5) * 1024); };
  int r = lane & 31, hl = lane >> 5;
  u32x2 qr[2][2]; unsigned ql[2];
  unsigned tk = (2 * (r & 15) + (r >> 4)) * 8 + hl * 32768;
  auto load_q = [&](int k) { const int qi = k & 1; const unsigned qs = WS_QK + 9 * (head >> 1) * 16384 + (8 * qb + 2 * t + (qi ^ hh)) * 256;
    if (k == 0) PIN(tk);
    if (k < 4) asm volatile("buffer_load_dwordx2 %0, %1, %2, %3 offen sc1" : "=&v"(qr[qi][k >> 1]) : "v"(tk), "s"(srd_ws), "s"(qs + (odd * 5 + (k >> 1)) * 16384) : "memory");
    else asm volatile("buffer_load_dword %0, %1, %2, %3 offen sc1" : "=&v"(ql[qi]) : "v"(tk), "s"(srd_ws), "s"(qs + 4 * 16384 + odd * 4) : "memory"); };
  int kb = 0, lb = 0, vb = 0; u32x16 qq; unsigned linc = 0, vinc = 0, ka = 0, la = 0, va = 0, cad = 0; u32x8 ev; f32x16 s0, s1; float mx0 = 0.f, mx1 = 0.f;
  const f32x16 z16 = {};
  auto tile_scores = [&](int co, int qi) {
    const u32x2 lo = *(const LAS u32x2*)(lds + co + kb), hi = *(const LAS u32x2*)(lds + co + kb + 1024);
    f32x16 s = mfma32(__builtin_bit_cast(s16x8, (u32x4){lo[0], lo[1], hi[0], hi[1]}), __builtin_bit_cast(s16x8, (u32x4){qq[4 * qi], qq[4 * qi + 1], qq[4 * qi + 2], qq[4 * qi + 3]}), z16);
    return mfma32k8((u32x2){*(const LAS unsigned*)(lds + co + lb), 0u}, (u32x2){hl ? 0u : ql[qi], 0u}, s); };
  auto set_ref = [&](int qi, float m) {
    const float n0 = -m; const unsigned h0 = __float_as_uint(n0) & 0xffff0000u; const float n1 = n0 - __uint_as_float(h0);
    const unsigned h1 = __float_as_uint(n1) & 0xffff0000u; const float n2 = n1 - __uint_as_float(h1);
    const unsigned w0 = (h0 >> 16) | h1, w1 = __float_as_uint(n2) >> 16;
    qq[8 + 4 * qi] = hl ? w0 : ql[qi]; qq[9 + 4 * qi] = hl ? w1 : 0u; qq[10 + 4 * qi] = hl ? w0 : 0u; qq[11 + 4 * qi] = hl ? w1 : ql[qi]; };
  auto att_addr = [&](int c0) {
    const int kbase = (c0 ? L_K1 : L_K0) + hh * 5120, vbase = (c0 ? L_V1 : L_V0) + hh * 4608;
    ka = lds0 + kbase + kb; la = hl ? lds0 + L_C + 64 : lds0 + kbase + lb; va = r < 18 ? lds0 + vbase + vb : lds0 + L_C + (r > 18 ? 16 : 0); };
  const int T = xc + 8 * (rr_ >> 2), cg = rr_ & 3;
  unsigned sc_[24];
  u32x2 klo, khi; unsigned kle = 0, one = 0;
  auto att_setup = [&](auto N_, auto G_) { constexpr int n = decltype(N_)::value, g = decltype(G_)::value, v = 4 * n + g;
    if constexpr (n >= 40 && n < 63 && g == 1) { constexpr int k = n - 40;
      const unsigned pb = WS_P + (T >> 2) * 16 * 144 * 1024 + (T & 3) * 1024, pd = WS_P + ((tg * 16 + j) * 144 + t * 4 + 2 * hh) * 1024, ob = WS_O + (8 * qb + 2 * t + hh) * 36 * 1024;
      sc_[k] = k == 0 ? lds0 + L_K1 + w * 5120 : k == 1 ? lds0 + L_V1 + w * 4608 : k == 2 ? WS_QK + (144 + 9 * (head >> 1) + 4 * odd) * 16384 + (8 + w) * 1024 : k == 3 ? WS_V + head * 73728 + (8 + w) * 4608
             : k == 4 ? pb + (9 * cg + w) * 4096 : k == 5 ? pb + (2 * w * 144 + (9 * cg + 8) * 4) * 1024 : k == 6 ? WS_WO + (9 * cg + w) * 18 * 1024 : k == 7 ? WS_G + 32 * T * 4
             : k == 8 ? WS_FC + (T >> 2) * 2048 : k == 9 ? (w == 0 ? WS_FC + (tg * 16 + j) * 128 : OOB) : k == 10 ? WS_WO + ((9 * cg + 8) * 18 + w) * 1024
             : k == 11 ? (w < 2 ? WS_WO + ((9 * cg + 8) * 18 + w + 16) * 1024 : OOB) : k < 18 ? pd + ksl[(k + 1) % 10] * 16384
             : k == 18 ? lds0 + (hh ? L_V0 : L_K0) + t * 3072 : k == 19 ? lds0 + (hh ? L_K0 : L_V0) + t * 3072 : k == 20 ? lds0 + L_FL + w * 4 : k == 21 ? ob + (head >> 1) * 2048 + odd * 512 : ob + 32768 + head * 128;
      asm volatile("" : "+s"(sc_[k])); }
    if constexpr (v == 4 * 64 + 2) { kb = (odd + 2 * hl) * 1024 + r * 8; PIN(kb); }
    if constexpr (v == 4 * 65 + 1) { lb = (odd ? 0 : 4096) + r * 8 + odd * 4; PIN(lb); }
    if constexpr (v == 4 * 65 + 2) { vb = hl * 16 + r * 64; PIN(vb); }
    if constexpr (v == 4 * 66 + 1) { linc = hl ? 0u : 1u; PIN(linc); }
    if constexpr (v == 4 * 66 + 2) { vinc = r < 18 ? 1u : 0u; PIN(vinc); }
    if constexpr (v == 4 * 67 + 1) { ka = lds0 + L_K0 + hh * 5120 + kb; PIN(ka); }
    if constexpr (v == 4 * 73 + 1) {
      asm volatile("" : "+v"(qr[0][0]), "+v"(qr[0][1]), "+v"(qr[1][0]), "+v"(qr[1][1]), "+v"(ql[0]), "+v"(ql[1]) :: "memory");
#pragma unroll
      for (int qi = 0; qi < 2; ++qi) { qq[4 * qi] = qr[qi][0][0]; qq[4 * qi + 1] = qr[qi][0][1]; qq[4 * qi + 2] = qr[qi][1][0]; qq[4 * qi + 3] = qr[qi][1][1]; }
      klo = *(const LAS u32x2*)(lds + L_K0 + kb); khi = *(const LAS u32x2*)(lds + L_K0 + kb + 1024); kle = *(const LAS unsigned*)(lds + L_K0 + lb); }
    if constexpr (v == 4 * 73 + 2) { qq[8] = hl ? 0u : ql[0]; qq[9] = 0u; qq[12] = hl ? 0u : ql[1]; qq[13] = 0u; PIN(qq); }
    if constexpr (v == 4 * 67 + 2) { la = hl ? lds0 + L_C + 64 : lds0 + L_K0 + hh * 5120 + lb; PIN(la); }
    if constexpr (v == 4 * 74 + 1) asm volatile("v_mfma_f32_32x32x16_bf16 %0, %1, %2, 0" : "=&v"(s0) : "v"((u32x4){klo[0], klo[1], khi[0], khi[1]}), "v"((u32x4){qq[0], qq[1], qq[2], qq[3]}));
    if constexpr (v == 4 * 69 + 1) { unsigned v1 = lds0 + L_V0 + hh * 4608 + vb, v2 = lds0 + L_C + (r > 18 ? 16 : 0); PIN(v1); PIN(v2); va = r < 18 ? v1 : v2; PIN(va); }
    if constexpr (v == 4 * 74 + 3) asm volatile("s_nop 1\n\tv_mfma_f32_32x32x8_bf16 %0, %1, %2, %0" : "+v"(s0) : "v"((u32x2){kle, kle}), "v"((u32x2){qq[8], qq[9]}));
    if constexpr (v == 4 * 75 + 1) asm volatile("v_mfma_f32_32x32x16_bf16 %0, %1, %2, 0" : "=&v"(s1) : "v"((u32x4){klo[0], klo[1], khi[0], khi[1]}), "v"((u32x4){qq[4], qq[5], qq[6], qq[7]}));
    if constexpr (v == 4 * 70 + 1) { c = lane & 15; q = lane >> 4; one = q == (c >> 2) ? ((c & 1) ? 0x3f800000u : 0x3f80u) : 0u; PIN(one); }
    if constexpr (v == 4 * 75 + 3) asm volatile("s_nop 1\n\tv_mfma_f32_32x32x8_bf16 %0, %1, %2, %0" : "+v"(s1) : "v"((u32x2){kle, kle}), "v"((u32x2){qq[12], qq[13]}));
    if constexpr (v == 4 * 72 + 1) { ev = (u32x8){(unsigned)lane * 16, (unsigned)c * 8, 0u, 0u, (c & 2) ? 0u : one, (c & 2) ? one : 0u, 0u, 0u}; PIN(ev); }
    if constexpr (v == 4 * 71 + 1) { unsigned c1 = (lane >> 2) * 1088 + lane * 16, c2 = 64 + (lane & 3) * 256; PIN(c1); PIN(c2); cad = lane < 16 ? c1 : c2; PIN(cad); }
#define MX5(m, s_, i_) asm volatile("v_max3_f32 %0, %0, %1, %2\n\tv_max3_f32 %0, %0, %3, %4" : "+v"(m) : "v"(s_[i_]), "v"(s_[(i_) + 1]), "v"(s_[(i_) + 2]), "v"(s_[(i_) + 3]))
    if constexpr (v == 4 * 76 + 3) { mx0 = s0[0]; MX5(mx0, s0, 0); }
    if constexpr (v > 4 * 76 + 3 && v <= 4 * 77 + 2) MX5(mx0, s0, 4 * (v - 4 * 77) + 4);
    if constexpr (v == 4 * 77 + 3) { mx0 = xhalf_max(mx0); PIN(mx0); }
    if constexpr (v == 4 * 78) { mx1 = s1[0]; MX5(mx1, s1, 0); }
    if constexpr (v > 4 * 78 && v <= 4 * 78 + 3) MX5(mx1, s1, 4 * (v - 4 * 78));
    if constexpr (v == 4 * 79) { mx1 = xhalf_max(mx1); PIN(mx1); }
    if constexpr (v == 4 * 79 + 1) { set_ref(0, mx0); PIN(qq); }
    if constexpr (v == 4 * 79 + 3) { set_ref(1, mx1); PIN(qq); }
  };
  u32x32 wfa, wfb;
  {
    f32x4 dc[3][4]; u32x4 pk[18];
    const unsigned pv = (t * 4 + 2 * hh) * 1024 + lane * 16, pb = WS_P + (tg * 16 + j) * 144 * 1024;
    int fo = po;
#define P_CVT(D, i_, tp, k) asm volatile("v_cvt_pk_bf16_f32 %0, %1, %2" : "=v"(pk[2 * (i_) + (tp)][k]) : "v"(D[2 * (tp) + ((k) >> 1)][2 * ((k) & 1)]), "v"(D[2 * (tp) + ((k) >> 1)][2 * ((k) & 1) + 1]))
    auto pstore = [&](int m) { __builtin_amdgcn_raw_buffer_store_b128(pk[m], rs_ws, pv, pb + ksl[m >> 1] * 16 * 1024 + (m & 1) * 1024, SC1); };
    G_BURST(accB, 4);
    sfor<0, 81>([&](auto N_) { constexpr int n = decltype(N_)::value, kp = d_k(n), i = d_i(n), f = n & 3, s = n >> 2, g = f + 3, si = d_sti(n), e = n - 1;
      if constexpr (g == 4 && n + 3 < 81) { fo = po; po = so; so = nextso(so); p = ringp(); SBAR(); vm_bar<d_cnt(s)>(); }
      if constexpr (kp == 0) MF0(dc[i % 3][0], a[n & 3], hf[kp][0]); else MF(dc[i % 3][0], a[n & 3], hf[kp][0]);
      SBAR();
      if constexpr (n + 3 < 81) a[(n + 3) & 3] = trfrag(p + (g & 3) * KC);
      SBAR(); att_setup(N_, IC<0>{}); SBAR();
      if constexpr (kp == 0) MF0(dc[i % 3][1], a[n & 3], hf[kp][1]); else MF(dc[i % 3][1], a[n & 3], hf[kp][1]);
      SBAR(); att_setup(N_, IC<1>{}); SBAR();
      if constexpr (n < 8) B_EXP(accB, n);
      if constexpr (e == 9) { G_CVT(accB, 0); G_CVT(accB, 1); }
      if constexpr (si >= 0) { P_CVT(dc[si % 3], si, d_stp(n), 0); P_CVT(dc[si % 3], si, d_stp(n), 1); }
      if constexpr (kp == 0) MF0(dc[i % 3][2], a[n & 3], hf[kp][2]); else MF(dc[i % 3][2], a[n & 3], hf[kp][2]);
      SBAR(); att_setup(N_, IC<2>{}); SBAR();
      if constexpr (n < 8) B_ADD(accB, n);
      if constexpr (n >= 1 && n <= 8) B_FIN(accB, n - 1);
      if constexpr (e == 9) { G_CVT(accB, 2); G_CVT(accB, 3); }
      if constexpr (si >= 0) { P_CVT(dc[si % 3], si, d_stp(n), 2); P_CVT(dc[si % 3], si, d_stp(n), 3); }
      SBAR();
      if constexpr (d_ring(n)) issue_d(s + 4, f >> 1, fo);
      if constexpr (d_kv(n)) issue_kv(0, s - 4);
      if constexpr (d_q(n)) load_q(d_qi(n));
      SBAR();
      if constexpr (kp == 0) MF0(dc[i % 3][3], a[n & 3], hf[kp][3]); else MF(dc[i % 3][3], a[n & 3], hf[kp][3]);
      SBAR(); att_setup(N_, IC<3>{}); SBAR();
      if constexpr (n < 8) B_RCP(accB, n);
      SBAR();
      constexpr int sm = d_stm(n);
      if constexpr (sm >= 0) pstore(sm);
      if constexpr (n + 3 < 81) lgkm<4>();
      SBAR();
      if constexpr (n >= 5 && n < 9) { constexpr int m = 2 * (n - 5); hf[6 + (m >> 2)][m & 3] = ldsr128(hr + ((6 + (m >> 2)) * 8 + (m & 3)) * 1024); hf[6 + (m >> 2)][(m & 3) + 1] = ldsr128(hr + ((6 + (m >> 2)) * 8 + (m & 3) + 1) * 1024); }
      if constexpr (e == 9) { const lds_ptr hw = lds + L_H + (64 + 2 * t) * 1024 + lane * 16 + hh * 8;
        *(LAS u32x2*)hw = (u32x2){hp[0][0], hp[0][1]}; *(LAS u32x2*)(hw + 1024) = (u32x2){hp[1][0], hp[1][1]}; }
      if constexpr (n == 13) { hf[8][0] = ldsr128(hr + 64 * 1024); hf[8][1] = ldsr128(hr + 65 * 1024); hf[8][2] = ldsr128(hr + 66 * 1024); hf[8][3] = ldsr128(hr + 67 * 1024); }
      SBAR();
    });
    asm volatile("s_nop 7" ::: "memory"); SBAR();
    P_CVT(dc[2], 8, 0, 0); P_CVT(dc[2], 8, 0, 1); P_CVT(dc[2], 8, 0, 2); P_CVT(dc[2], 8, 0, 3);
    P_CVT(dc[2], 8, 1, 0); P_CVT(dc[2], 8, 1, 1); P_CVT(dc[2], 8, 1, 2); P_CVT(dc[2], 8, 1, 3);
#pragma unroll
    for (int i = 0; i < ATT_NC; ++i)
#pragma unroll
      for (int e = 0; e < 4; ++e) { if (i < 8) wfa[4 * i + e] = pk[18 - ATT_NC + i][e]; else wfb[4 * (i - 8) + e] = pk[18 - ATT_NC + i][e]; }
  }
  u32x16 wfc; u32x4 wfd; f32x16 bsum; u32x2 gg; unsigned redo;
  {
    asm volatile("s_barrier" ::: "memory");
    { const unsigned cv = (lane < 16 && (lane & 1)) ? 0u : 0x3f803f80u; *(LAS u32x4*)(lds + L_C + cad) = (u32x4){cv, cv, cv, cv}; }
    asm volatile(ATT_FULL_ASM : ATT_W_WF0(wfa), ATT_W_WF1(wfb), ATT_W_WF2(wfc), ATT_W_WF3(wfd), ATT_W_BS(bsum), ATT_W_GG(gg), ATT_R_KA(ka), ATT_R_LA(la), ATT_R_VA(va), [t0] "=&s"(redo), "+" ATT_R_Q(qq), "+" ATT_R_LINC(linc), "+" ATT_R_VINC(vinc)
                 : ATT_R_IDX(0xDDDDu), ATT_R_E(ev), [srd] "s"(srd_ws), [tag] "s"(tag),
                   [kinc] "s"(10240), [vinc] "s"(9216), [kjmp] "s"(L_K1 - L_K0 - 3 * 10240), [vjmp] "s"(L_V1 - L_V0 - 3 * 9216),
                   [kdst] "s"(sc_[0]), [vdst] "s"(sc_[1]), [ksrc] "s"(sc_[2]), [vsrc] "s"(sc_[3]), [psrc] "s"(sc_[4]), [p9src] "s"(sc_[5]), [wsrc] "s"(sc_[6]), [gsrc] "s"(sc_[7]),
                   [fsrc] "s"(sc_[8]), [fdst] "s"(sc_[9]), [w9src] "s"(sc_[10]), [w9last] "s"(sc_[11]),
                   [pd3] "s"(sc_[12]), [pd4] "s"(sc_[13]), [pd5] "s"(sc_[14]), [pd6] "s"(sc_[15]), [pd7] "s"(sc_[16]), [pd8] "s"(sc_[17]),
                   [xw] "s"(sc_[18]), [xr] "s"(sc_[19]), [flw] "s"(sc_[20]), [flr] "s"(lds0 + L_FL), [osm] "s"(sc_[21]), [osl] "s"(sc_[22])
                 : ATT_SEG_CLOB, ATT_FULL_CLOB, ATT_O_CLOB, "scc", "vcc", "memory");
    SBAR();
  }
  if (redo) {
    lane = fresh_lane(); r = lane & 31; hl = lane >> 5;
    asm volatile("s_barrier" ::: "memory");
#pragma unroll
    for (int k = 0; k < 10; ++k) issue_kv(0, k);
    asm volatile("s_waitcnt vmcnt(0)\n\ts_barrier" ::: "memory");
    kb = (odd + 2 * hl) * 1024 + r * 8; lb = (odd ? 0 : 4096) + r * 8 + odd * 4; vb = hl * 16 + r * 64; linc = hl ? 0u : 1u; vinc = r < 18 ? 1u : 0u;
    ql[0] = qq[8]; ql[1] = qq[12];
    float m0 = -INFINITY, m1 = -INFINITY;
    for (int tl = 0; tl < 64; ++tl) { const int co = ((tl & 32) ? L_K1 : L_K0) + ((tl >> 2) & 7) * 5120 + (tl & 3) * 256;
      m0 = __builtin_fmaxf(m0, rowmax16(tile_scores(co, 0))); m1 = __builtin_fmaxf(m1, rowmax16(tile_scores(co, 1))); }
    set_ref(0, xhalf_max(m0)); set_ref(1, xhalf_max(m1));
    f32x32 oo; ATT_ZERO_O(oo);
#pragma nounroll
    for (int c0 = 0; c0 < 2; ++c0) { unsigned cnt = 4; att_addr(c0);
      asm volatile(ATT_SEG_ASM : ATT_R_O(oo), ATT_R_KA(ka), ATT_R_LA(la), ATT_R_VA(va), [cnt] "+s"(cnt)
                   : ATT_R_Q(qq), ATT_R_LINC(linc), ATT_R_VINC(vinc), ATT_R_IDX(0xDDDDu), [kinc] "s"(10240), [vinc] "s"(9216) : ATT_SEG_CLOB, "scc", "memory"); SBAR(); }
    asm volatile("s_barrier" ::: "memory");
#pragma unroll
    for (int e = 0; e < 3; ++e) *(LAS f32x4*)(lds + (hh ? L_V0 : L_K0) + (t * 3 + e) * 1024 + lane * 16) = (f32x4){oo[16 + 4 * e], oo[17 + 4 * e], oo[18 + 4 * e], oo[19 + 4 * e]};
    asm volatile("s_waitcnt lgkmcnt(0)\n\ts_barrier" ::: "memory");
    f32x4 mine[3];
#pragma unroll
    for (int e = 0; e < 3; ++e) mine[e] = (f32x4){oo[4 * e], oo[4 * e + 1], oo[4 * e + 2], oo[4 * e + 3]} + *(const LAS f32x4*)(lds + (hh ? L_K0 : L_V0) + (t * 3 + e) * 1024 + lane * 16);
    auto rr = __builtin_amdgcn_permlane32_swap(__float_as_uint(mine[2][2]), __float_as_uint(mine[2][2]), false, false);
    const float rl = __builtin_amdgcn_rcpf(__uint_as_float(rr[0]));
    auto pkc = [&](float x0, float x1) { return __builtin_elementwise_min(cvtpk(x0 * rl, x1 * rl), 0xfffffffeu); };
    o_addr();
    store_o((u32x4){pkc(mine[0][0], mine[0][1]), pkc(mine[0][2], mine[0][3]), pkc(mine[1][0], mine[1][1]), pkc(mine[1][2], mine[1][3])}, pkc(mine[2][0], mine[2][1]));
    unsigned t0;
    asm volatile(ATT_RELOAD_ASM : ATT_O_WF0(wfa), ATT_O_WF1(wfb), ATT_W_WF2(wfc), ATT_W_WF3(wfd), [t0] "=&s"(t0)
                 : [vo] "v"(lane * 16), [srd] "s"(srd_ws), [wsrc] "s"(sc_[6]), [w9src] "s"(sc_[10]), [w9last] "s"(sc_[11]) : "scc", "memory");
  }
  lane = fresh_lane(); c = lane & 15; q = lane >> 4;
  const float g0 = __uint_as_float(gg[0]), g1 = __uint_as_float(gg[1]);
  s16x8 wf[21]; f32x4 bs[2], bx[2];
#pragma unroll
  for (int k = 0; k < 21; ++k) { u32x4 v;
#pragma unroll
    for (int e = 0; e < 4; ++e) v[e] = k < 8 ? wfa[4 * k + e] : k < 16 ? wfb[4 * (k & 7) + e] : k < 20 ? wfc[4 * (k & 3) + e] : wfd[e];
    wf[k] = __builtin_bit_cast(s16x8, v); }
#pragma unroll
  for (int e = 0; e < 4; ++e) { bs[0][e] = (1.0f - g0) * bsum[e]; bs[1][e] = (1.0f - g1) * bsum[4 + e]; bx[0][e] = (1.0f - g0) * bsum[8 + e]; bx[1][e] = (1.0f - g1) * bsum[12 + e]; }
  asm volatile("" : "+v"(bs[0]), "+v"(bs[1]), "+v"(bx[0]), "+v"(bx[1]));
  { u32x4 ov[3][2];
    f32x4 x0 = z4, x1 = z4;
    const unsigned ob = WS_O + (T * 18 + w) * 2048;
    auto fetch = [&](int k) {
      if (k < 2) { ov[k][0] = __builtin_amdgcn_raw_buffer_load_b128(rs_ws, lane * 16, ob + k * 16384, SC1); ov[k][1] = __builtin_amdgcn_raw_buffer_load_b128(rs_ws, lane * 16, ob + k * 16384 + 1024, SC1); }
      else {
#pragma unroll
        for (int e = 0; e < 4; ++e) { const u32x2 v = __builtin_amdgcn_raw_buffer_load_b64(rs_ws, q * 512 + c * 8 + e * 128, w < 2 ? ob + 32768 : OOB, SC1); ov[2][0][e] = v[0]; ov[2][1][e] = v[1]; } } };
    fetch(0); fetch(1); fetch(2);
#pragma nounroll
    for (unsigned spin = 0; spin < (1u << 16); ++spin) {
      bool miss[3];
#pragma unroll
      for (int k = 0; k < 3; ++k) { const u32x4 m4 = __builtin_elementwise_max(ov[k][0], ov[k][1]);
        miss[k] = __any(__builtin_elementwise_max(__builtin_elementwise_max(m4[0], m4[1]), __builtin_elementwise_max(m4[2], m4[3])) == ~0u); }
      if (!(miss[0] || miss[1] || miss[2])) break;
      __builtin_amdgcn_s_sleep(2); asm volatile("" ::: "memory");
#pragma unroll
      for (int k = 0; k < 3; ++k) if (miss[k]) fetch(k); }
#pragma unroll
    for (int k = 0; k < 3; ++k) {
      *(LAS u32x4*)(lds + (w + 8 * k) * 2048 + lane * 16) = ov[k][0]; *(LAS u32x4*)(lds + (w + 8 * k) * 2048 + 1024 + lane * 16) = ov[k][1];
      x0 = mfma16(wf[18 + k], __builtin_bit_cast(s16x8, ov[k][0]), x0); x1 = mfma16(wf[18 + k], __builtin_bit_cast(s16x8, ov[k][1]), x1); }
    *(LAS f32x4*)(lds + 65536 + w * 2048 + lane * 16) = g0 * x0 + bx[0];
    *(LAS f32x4*)(lds + 65536 + w * 2048 + 1024 + lane * 16) = g1 * x1 + bx[1];
    asm volatile("s_waitcnt lgkmcnt(0)\n\ts_barrier" ::: "memory"); }
  const __amdgpu_buffer_rsrc_t rs_out = __builtin_amdgcn_make_buffer_rsrc(out, 0, S_ * D_ * 2, 0x20000);
  auto put = [&](const f32x4& v, int ts, int dt) { const u32x2 ov = {cvtpk(v[0], v[1]), cvtpk(v[2], v[3])};
    __builtin_amdgcn_raw_buffer_store_b64(ov, rs_out, (2 * c * 576 + 4 * q) * 2, ((32 * T + ts) * 576 + 16 * dt) * 2, 0); };
  s16x8 of[18][2];
#pragma unroll
  for (int k = 0; k < 18; ++k) { of[k][0] = ldsr128(lds + k * 2048 + lane * 16); of[k][1] = ldsr128(lds + k * 2048 + 1024 + lane * 16); }
  SBAR();
  f32x4 a0 = z4, a1 = z4;
#pragma unroll
  for (int k = 0; k < 18; ++k) { a0 = mfma16(wf[k], of[k][0], a0); a1 = mfma16(wf[k], of[k][1], a1); }
  put(g0 * a0 + bs[0], 0, 9 * cg + w); put(g1 * a1 + bs[1], 1, 9 * cg + w);
  if (w >= 6) { f32x4 v = z4;
#pragma unroll
    for (int k = 0; k < 8; ++k) v += *(const LAS f32x4*)(lds + 65536 + k * 2048 + (w - 6) * 1024 + lane * 16);
    put(v, w - 6, 9 * cg + 8); }
  asm volatile("L_code_end:");
}

extern "C" void kernel_launch(void* const* d_in, const int* in_sizes, int n_in, void* d_out, int out_size, void* d_ws, size_t ws_size, hipStream_t stream) {
  (void)hipFuncSetAttribute((const void*)k_all, hipFuncAttributeMaxDynamicSharedMemorySize, L_TOTAL);
  k_all<<<256, 512, L_TOTAL, stream>>>((const char*)d_in[0], (const char*)d_in[1], (const char*)d_in[2], (const char*)d_in[4], (const char*)d_in[5], (const char*)d_in[6], (const char*)d_in[7], (char*)d_ws, (char*)d_out);
}
```
